```python
import torch

def _softmax(x, axis=-1):
    return torch.softmax(x, dim=axis)
def _gelu(x):
    return torch.nn.functional.gelu(x, approximate="tanh")
def _silu(x):
    return torch.nn.functional.silu(x)
def _sigmoid(x):
    return torch.sigmoid(x)
def _layernorm(x, axis=-1, eps=1e-5):
    m = x.mean(dim=axis, keepdim=True)
    v = x.var(dim=axis, keepdim=True, unbiased=False)
    return (x - m) / torch.sqrt(v + eps)
def _scatter_add(tgt, idx, src, axis=0):
    return tgt.clone().index_add_(axis, idx, src)
def _segment_sum(src, seg, n):
    out = torch.zeros((n,) + src.shape[1:], dtype=src.dtype)
    return out.index_add_(0, seg, src)
def _pad(x, pw):
    flat = tuple(v for pair in reversed(pw) for v in pair)
    return torch.nn.functional.pad(x, flat)

kernel_name = 'concat_heads__sliding_block_cached__sliding_block_cached__decode'

def setup_inputs():

    _w = (lambda a: (lambda t: t.to(torch.bfloat16) if t.is_floating_point() else t)(torch.as_tensor(a)))
    def _A_setup():
        B, Sq, D = 2, 1, 512
        Hq, Hkv, Sk, Dh = 32, 32, 8192, 128
        assert Hq % Hkv == 0, (Hq, Hkv)
        return {
            "x": _w((lambda *s: torch.randn(*s, dtype=torch.bfloat16) * 0.8)(B, Sq, D)),
            "k_cache": _w((lambda *s: torch.randn(*s, dtype=torch.bfloat16) * 1.6)(B, Hkv, Sk, Dh)),
            "v_cache": _w((lambda *s: torch.randn(*s, dtype=torch.bfloat16) * 0.2)(B, Hkv, Sk, Dh)),
            "wq": _w((lambda *s: torch.randn(*s, dtype=torch.bfloat16) * 1.6)(D, Hq * Dh) * (D ** -0.5)),
            "wo": _w((lambda *s: torch.randn(*s, dtype=torch.bfloat16) * 0.2)(Hq * Dh, D) * ((Hq * Dh) ** -0.5)),
        }
    _A_in = _A_setup()

    def _B_setup():
        B, Sq, D = 2, 1, 512
        Hq, Hkv, Sk, Dh = 32, 32, 8192, 128
        assert Hq % Hkv == 0, (Hq, Hkv)
        return {
            "x": _w((lambda *s: torch.randn(*s, dtype=torch.bfloat16) * 0.8)(B, Sq, D)),
            "k_cache": _w((lambda *s: torch.randn(*s, dtype=torch.bfloat16) * 1.6)(B, Hkv, Sk, Dh)),
            "v_cache": _w((lambda *s: torch.randn(*s, dtype=torch.bfloat16) * 0.2)(B, Hkv, Sk, Dh)),
            "wq": _w((lambda *s: torch.randn(*s, dtype=torch.bfloat16) * 1.6)(D, Hq * Dh) * (D ** -0.5)),
            "wo": _w((lambda *s: torch.randn(*s, dtype=torch.bfloat16) * 0.2)(Hq * Dh, D) * ((Hq * Dh) ** -0.5)),
        }
    _B_in = _B_setup()

    return {
        "A_x": _A_in["x"],
        "A_k_cache": _A_in["k_cache"],
        "A_v_cache": _A_in["v_cache"],
        "A_wq": _A_in["wq"],
        "A_wo": _A_in["wo"],
        "B_x": _B_in["x"],
        "B_k_cache": _B_in["k_cache"],
        "B_v_cache": _B_in["v_cache"],
        "B_wq": _B_in["wq"],
        "B_wo": _B_in["wo"],
    }

def reference(A_x, A_k_cache, A_v_cache, A_wq, A_wo, B_x, B_k_cache, B_v_cache, B_wq, B_wo):
    def _A_ref(x, k_cache, v_cache, wq, wo):
        B, Sq, D = x.shape
        Hq, Dh = 32, 128
        Hkv, Sk = k_cache.shape[1], k_cache.shape[2]
        W = min(4096, max(256, Sk // 4))
        rep = Hq // Hkv
        q = (lambda x, *d: x.permute(*d))((x @ wq).reshape(B, Sq, Hq, Dh), 0, 2, 1, 3)
        k = (lambda x, n, axis: torch.repeat_interleave(x, n, dim=axis))(k_cache[:, :, Sk - W:, :], rep, axis=1)
        v = (lambda x, n, axis: torch.repeat_interleave(x, n, dim=axis))(v_cache[:, :, Sk - W:, :], rep, axis=1)
        scores = torch.einsum("bhqd,bhkd->bhqk", q, k) * (Dh ** -0.5)
        p = _softmax(scores, axis=-1)
        o = torch.einsum("bhqk,bhkd->bhqd", p, v)
        return (lambda x, *d: x.permute(*d))(o, 0, 2, 1, 3).reshape(B, Sq, Hq * Dh) @ wo

    def _B_ref(x, k_cache, v_cache, wq, wo):
        B, Sq, D = x.shape
        Hq, Dh = 32, 128
        Hkv, Sk = k_cache.shape[1], k_cache.shape[2]
        W = min(4096, max(256, Sk // 4))
        rep = Hq // Hkv
        q = (lambda x, *d: x.permute(*d))((x @ wq).reshape(B, Sq, Hq, Dh), 0, 2, 1, 3)
        k = (lambda x, n, axis: torch.repeat_interleave(x, n, dim=axis))(k_cache[:, :, Sk - W:, :], rep, axis=1)
        v = (lambda x, n, axis: torch.repeat_interleave(x, n, dim=axis))(v_cache[:, :, Sk - W:, :], rep, axis=1)
        scores = torch.einsum("bhqd,bhkd->bhqk", q, k) * (Dh ** -0.5)
        p = _softmax(scores, axis=-1)
        o = torch.einsum("bhqk,bhkd->bhqd", p, v)
        return (lambda x, *d: x.permute(*d))(o, 0, 2, 1, 3).reshape(B, Sq, Hq * Dh) @ wo

    a = _A_ref(A_x, A_k_cache, A_v_cache, A_wq, A_wo)
    b = _B_ref(B_x, B_k_cache, B_v_cache, B_wq, B_wo)
    return torch.concatenate([a, b], axis=-1)
```

```cpp
#include <hip/hip_runtime.h>
#include <hip/hip_bf16.h>
#include <stdint.h>

typedef unsigned v4u __attribute__((ext_vector_type(4)));
typedef unsigned v2u __attribute__((ext_vector_type(2)));
typedef float f32x4 __attribute__((ext_vector_type(4)));
typedef __bf16 bf16x2 __attribute__((ext_vector_type(2)));
#define GAS __attribute__((address_space(1)))

constexpr int SK = 8192, WIN = 2048, DH = 128, NH = 32, DM = 512, HD = 4096;

struct Params {
  const unsigned short* x[2];
  const unsigned short* k[2];
  const unsigned short* v[2];
  const unsigned short* wq[2];
  const unsigned short* wo[2];
  unsigned short* out;
  float* ws;
};

__device__ __forceinline__ float dot2(unsigned a, unsigned b, float acc) {
  return __builtin_amdgcn_fdot2_f32_bf16(__builtin_bit_cast(bf16x2, a), __builtin_bit_cast(bf16x2, b), acc, false);
}
__device__ __forceinline__ unsigned cvtpk(float lo, float hi) {
  bf16x2 r; r.x = (__bf16)lo; r.y = (__bf16)hi; return __builtin_bit_cast(unsigned, r);
}
__device__ __forceinline__ float rbf(float x) { return (float)(__bf16)x; }
template <int CTRL> __device__ __forceinline__ float dpp(float x) {
  return __builtin_bit_cast(float, __builtin_amdgcn_mov_dpp(__builtin_bit_cast(int, x), CTRL, 0xf, 0xf, true));
}
constexpr int XOR1 = 0xB1, XOR2 = 0x4E, XOR7 = 0x141, XOR8 = 0x128, ROR4 = 0x124;
__device__ __forceinline__ float xrow16_max(float x) {
  auto s = __builtin_amdgcn_permlane16_swap(__float_as_uint(x), __float_as_uint(x), false, false);
  x = fmaxf(__uint_as_float(s[0]), __uint_as_float(s[1]));
  auto t = __builtin_amdgcn_permlane32_swap(__float_as_uint(x), __float_as_uint(x), false, false);
  return fmaxf(__uint_as_float(t[0]), __uint_as_float(t[1]));
}
__device__ __forceinline__ float xrow16_sum(float x) {
  auto s = __builtin_amdgcn_permlane16_swap(__float_as_uint(x), __float_as_uint(x), false, false);
  x = __uint_as_float(s[0]) + __uint_as_float(s[1]);
  auto t = __builtin_amdgcn_permlane32_swap(__float_as_uint(x), __float_as_uint(x), false, false);
  return __uint_as_float(t[0]) + __uint_as_float(t[1]);
}
__device__ __forceinline__ float lo16(unsigned u) { return __builtin_bit_cast(float, u << 16); }
__device__ __forceinline__ float hi16(unsigned u) { return __builtin_bit_cast(float, u & 0xffff0000u); }
__device__ __forceinline__ v4u ldnt(const GAS v4u* p) { return __builtin_nontemporal_load(p); }
__device__ __forceinline__ v4u ldpl(const GAS v4u* p) { return *p; }
#define SB() __builtin_amdgcn_sched_barrier(0)
#define BAR() do { asm volatile("s_waitcnt lgkmcnt(0)" ::: "memory"); __builtin_amdgcn_s_barrier(); asm volatile("" ::: "memory"); } while (0)

template <int J> struct BC { static constexpr int v = 0x150 + J; };

__global__ void __launch_bounds__(512, 2) k_attn(Params P) {
  __shared__ __attribute__((aligned(16))) float lds[8 * 128 + 8 * 128 + 64];
  float* qpart = lds;
  float* opart = lds + 1024;
  float* red = lds + 2048;
  const int tid = threadIdx.x, lane = tid & 63, w = __builtin_amdgcn_readfirstlane(tid >> 6);
  const int r = lane >> 4, c = lane & 15;
  const int bid = blockIdx.x, xcd = bid & 7, jj = bid >> 3, hp = xcd * 8 + (jj >> 2);
  const int br = hp >> 5, h = hp & 31, b = (jj >> 1) & 1, sp = jj & 1;

  const GAS v4u* xp = (const GAS v4u*)(P.x[br] + b * DM + 64 * w + 16 * r);
  const GAS v4u* wqp = (const GAS v4u*)(P.wq[br] + (size_t)(64 * w + 16 * r) * HD + h * DH + 8 * c);
  const size_t kvoff = ((size_t)(b * NH + h) * SK + (SK - WIN) + sp * 1024 + w * 128) * DH;
  const GAS v4u* kp = (const GAS v4u*)(P.k[br] + kvoff) + lane;
  const GAS v4u* vp = (const GAS v4u*)(P.v[br] + kvoff) + lane;
  const int g = lane >> 3, c8 = lane & 7;
  const GAS v4u* wop = (const GAS v4u*)(P.wo[br] + (size_t)(h * DH + 16 * g) * DM + 64 * w + 8 * c8);

  v4u xa = ldpl(xp), xb = ldpl(xp + 1);
  v4u A[16], K[32];
#pragma unroll
  for (int j = 0; j < 16; ++j) A[j] = ldpl(wqp + j * (HD / 8));
  SB();
#pragma unroll
  for (int j = 0; j < 32; ++j) K[j] = ldnt(kp + j * 64);
  SB();
  float qa[8];
#pragma unroll
  for (int i = 0; i < 8; ++i) qa[i] = 0.f;
#pragma unroll
  for (int j = 0; j < 16; j += 2) {
    const unsigned xx = j < 8 ? xa[j / 2] : xb[j / 2 - 4];
#pragma unroll
    for (int k = 0; k < 4; ++k) {
      const unsigned lo = __builtin_amdgcn_perm(A[j + 1][k], A[j][k], 0x05040100u);
      const unsigned hi = __builtin_amdgcn_perm(A[j + 1][k], A[j][k], 0x07060302u);
      qa[2 * k] = dot2(lo, xx, qa[2 * k]);
      qa[2 * k + 1] = dot2(hi, xx, qa[2 * k + 1]);
    }
    SB();
    A[j] = ldnt(vp + j * 64);
    A[j + 1] = ldnt(vp + (j + 1) * 64);
    SB();
  }
#pragma unroll
  for (int i = 0; i < 8; ++i) qa[i] = xrow16_sum(qa[i]);
  if (r == 0) {
    *(f32x4*)(qpart + w * 128 + 8 * c) = f32x4{qa[0], qa[1], qa[2], qa[3]};
    *(f32x4*)(qpart + w * 128 + 8 * c + 4) = f32x4{qa[4], qa[5], qa[6], qa[7]};
  }
  BAR();
  f32x4 q0 = {0.f, 0.f, 0.f, 0.f}, q1 = {0.f, 0.f, 0.f, 0.f};
#pragma unroll
  for (int ww = 0; ww < 8; ++ww) {
    q0 += *(const f32x4*)(qpart + ww * 128 + 8 * c);
    q1 += *(const f32x4*)(qpart + ww * 128 + 8 * c + 4);
  }
  const v4u q = {cvtpk(q0[0], q0[1]), cvtpk(q0[2], q0[3]), cvtpk(q1[0], q1[1]), cvtpk(q1[2], q1[3])};
  float s[2];
#pragma unroll
  for (int hf = 0; hf < 2; ++hf) {
    float x[16];
#pragma unroll
    for (int j = 0; j < 16; ++j) {
      const int jj2 = hf * 16 + j;
      float d = 0.f;
#pragma unroll
      for (int k = 0; k < 4; ++k) d = dot2(K[jj2][k], q[k], d);
      x[j] = d;
      SB();
      if (hf == 0) K[jj2] = ldnt(vp + (16 + j) * 64);
      else K[jj2] = ldpl(wop + j * (DM / 8));
      SB();
    }
#define TR_STEP(HALF, CTRL, BIT) _Pragma("unroll") for (int hh = 0; hh < (HALF); ++hh) { \
      const float lo_ = x[hh], hi_ = x[hh + (HALF)]; const float t1_ = lo_ + dpp<CTRL>(lo_), t2_ = hi_ + dpp<CTRL>(hi_); \
      x[hh] = (c & (BIT)) ? t2_ : t1_; }
    TR_STEP(8, XOR8, 8) TR_STEP(4, XOR7, 4) TR_STEP(2, XOR2, 2) TR_STEP(1, XOR1, 1)
#undef TR_STEP
    s[hf] = rbf(rbf(x[0]) * 0.08838834764831845f);
  }
  float m = fmaxf(s[0], s[1]);
  m = fmaxf(m, dpp<XOR1>(m)); m = fmaxf(m, dpp<XOR2>(m)); m = fmaxf(m, dpp<XOR7>(m)); m = fmaxf(m, dpp<XOR8>(m));
  m = xrow16_max(m);
  if (lane == 0) red[w] = m;
  BAR();
  {
    const f32x4 m0 = *(const f32x4*)(red), m1 = *(const f32x4*)(red + 4);
    m = fmaxf(fmaxf(fmaxf(m0[0], m0[1]), fmaxf(m0[2], m0[3])), fmaxf(fmaxf(m1[0], m1[1]), fmaxf(m1[2], m1[3])));
  }
  constexpr float L2E = 1.4426950408889634f;
  float p[2];
  p[0] = __builtin_amdgcn_exp2f((s[0] - m) * L2E);
  p[1] = __builtin_amdgcn_exp2f((s[1] - m) * L2E);
  float l = p[0] + p[1];
  l += dpp<XOR1>(l); l += dpp<XOR2>(l); l += dpp<XOR7>(l); l += dpp<XOR8>(l);
  l = xrow16_sum(l);
  if (lane == 0) red[16 + w] = l;
  float acc[8];
#pragma unroll
  for (int i = 0; i < 8; ++i) acc[i] = 0.f;
#define PV(J, REG) { const float pj = dpp<0x150 + ((J) & 15)>(p[(J) >> 4]); \
    _Pragma("unroll") for (int k = 0; k < 4; ++k) { acc[2 * k] = fmaf(pj, lo16(REG[k]), acc[2 * k]); acc[2 * k + 1] = fmaf(pj, hi16(REG[k]), acc[2 * k + 1]); } SB(); }
  PV(0, A[0]) PV(1, A[1]) PV(2, A[2]) PV(3, A[3]) PV(4, A[4]) PV(5, A[5]) PV(6, A[6]) PV(7, A[7])
  PV(8, A[8]) PV(9, A[9]) PV(10, A[10]) PV(11, A[11]) PV(12, A[12]) PV(13, A[13]) PV(14, A[14]) PV(15, A[15])
  PV(16, K[0]) PV(17, K[1]) PV(18, K[2]) PV(19, K[3]) PV(20, K[4]) PV(21, K[5]) PV(22, K[6]) PV(23, K[7])
  PV(24, K[8]) PV(25, K[9]) PV(26, K[10]) PV(27, K[11]) PV(28, K[12]) PV(29, K[13]) PV(30, K[14]) PV(31, K[15])
#undef PV
#pragma unroll
  for (int i = 0; i < 8; ++i) acc[i] = xrow16_sum(acc[i]);
  if (r == 0) {
    *(f32x4*)(opart + w * 128 + 8 * c) = f32x4{acc[0], acc[1], acc[2], acc[3]};
    *(f32x4*)(opart + w * 128 + 8 * c + 4) = f32x4{acc[4], acc[5], acc[6], acc[7]};
  }
  BAR();
  float o2[2] = {0.f, 0.f};
#pragma unroll
  for (int ww = 0; ww < 8; ++ww) {
    const float2 t = *(const float2*)(opart + ww * 128 + 2 * lane);
    o2[0] += t.x; o2[1] += t.y;
  }
  float y[8];
#pragma unroll
  for (int i = 0; i < 8; ++i) y[i] = 0.f;
#pragma unroll
  for (int j = 0; j < 16; ++j) {
    const float oj = __shfl(o2[j & 1], (lane & ~7) + (j >> 1), 64);
#pragma unroll
    for (int k = 0; k < 4; ++k) {
      y[2 * k] = fmaf(oj, lo16(K[16 + j][k]), y[2 * k]);
      y[2 * k + 1] = fmaf(oj, hi16(K[16 + j][k]), y[2 * k + 1]);
    }
  }
#pragma unroll
  for (int i = 0; i < 8; ++i) { y[i] += dpp<XOR8>(y[i]); y[i] = xrow16_sum(y[i]); }
  const int grp = br * 2 + b, pidx = h * 2 + sp;
  float* yp = P.ws + 1024 + (size_t)(grp * 64 + pidx) * 512;
  if (lane < 8) {
    *(f32x4*)(yp + 64 * w + 8 * c8) = f32x4{y[0], y[1], y[2], y[3]};
    *(f32x4*)(yp + 64 * w + 8 * c8 + 4) = f32x4{y[4], y[5], y[6], y[7]};
  }
  if (tid == 0) {
    float L = 0.f;
#pragma unroll
    for (int ww = 0; ww < 8; ++ww) L += red[16 + ww];
    *(float2*)(P.ws + (grp * 64 + pidx) * 2) = make_float2(m, L);
  }
}

__global__ void __launch_bounds__(64) k_comb(const float* ws, unsigned short* out) {
  const int lane = threadIdx.x, g = lane >> 3, c = lane & 7;
  const int grp = blockIdx.x >> 4, cb = blockIdx.x & 15;
  f32x4 ml[8], yv[8];
#pragma unroll
  for (int i = 0; i < 8; ++i) {
    const int p = 8 * i + g;
    ml[i] = *(const f32x4*)(ws + (grp * 64 + (p & ~1)) * 2);
    yv[i] = *(const f32x4*)(ws + 1024 + (size_t)(grp * 64 + p) * 512 + cb * 32 + 4 * c);
  }
  constexpr float L2E = 1.4426950408889634f;
  f32x4 a = {0.f, 0.f, 0.f, 0.f};
#pragma unroll
  for (int i = 0; i < 8; ++i) {
    const float M = fmaxf(ml[i][0], ml[i][2]);
    const float e0 = __builtin_amdgcn_exp2f((ml[i][0] - M) * L2E), e1 = __builtin_amdgcn_exp2f((ml[i][2] - M) * L2E);
    const float L = e0 * ml[i][1] + e1 * ml[i][3];
    const float wt = ((g & 1) ? e1 : e0) / L;
    a += wt * yv[i];
  }
#pragma unroll
  for (int i = 0; i < 4; ++i) { a[i] += dpp<XOR8>(a[i]); a[i] = xrow16_sum(a[i]); }
  if (lane < 8) {
    const int br = grp >> 1, b = grp & 1;
    v2u o = {cvtpk(a[0], a[1]), cvtpk(a[2], a[3])};
    *(v2u*)(out + b * 1024 + br * 512 + cb * 32 + 4 * c) = o;
  }
}

extern "C" void kernel_launch(void* const* d_in, const int* in_sizes, int n_in,
                              void* d_out, int out_size, void* d_ws, size_t ws_size,
                              hipStream_t stream) {
  Params P;
  for (int i = 0; i < 2; ++i) {
    P.x[i] = (const unsigned short*)d_in[5 * i + 0];
    P.k[i] = (const unsigned short*)d_in[5 * i + 1];
    P.v[i] = (const unsigned short*)d_in[5 * i + 2];
    P.wq[i] = (const unsigned short*)d_in[5 * i + 3];
    P.wo[i] = (const unsigned short*)d_in[5 * i + 4];
  }
  P.out = (unsigned short*)d_out;
  P.ws = (float*)d_ws;
  hipLaunchKernelGGL(k_attn, dim3(256), dim3(512), 0, stream, P);
  hipLaunchKernelGGL(k_comb, dim3(64), dim3(64), 0, stream, (const float*)d_ws, (unsigned short*)d_out);
}
```

```cpp
#include <hip/hip_runtime.h>
#include <hip/hip_bf16.h>
#include <stdint.h>

typedef unsigned v4u __attribute__((ext_vector_type(4)));
typedef unsigned v2u __attribute__((ext_vector_type(2)));
typedef float f32x4 __attribute__((ext_vector_type(4)));
typedef __bf16 bf16x2 __attribute__((ext_vector_type(2)));
#define GAS __attribute__((address_space(1)))

constexpr int SK = 8192, WIN = 2048, DH = 128, NH = 32, DM = 512, HD = 4096;

struct Params {
  const unsigned short* x[2];
  const unsigned short* k[2];
  const unsigned short* v[2];
  const unsigned short* wq[2];
  const unsigned short* wo[2];
  unsigned short* out;
  float* ws;
};

__device__ __forceinline__ float dot2(unsigned a, unsigned b, float acc) {
  return __builtin_amdgcn_fdot2_f32_bf16(__builtin_bit_cast(bf16x2, a), __builtin_bit_cast(bf16x2, b), acc, false);
}
__device__ __forceinline__ unsigned cvtpk(float lo, float hi) {
  bf16x2 r; r.x = (__bf16)lo; r.y = (__bf16)hi; return __builtin_bit_cast(unsigned, r);
}
__device__ __forceinline__ float rbf(float x) { return (float)(__bf16)x; }
template <int CTRL> __device__ __forceinline__ float dpp(float x) {
  return __builtin_bit_cast(float, __builtin_amdgcn_mov_dpp(__builtin_bit_cast(int, x), CTRL, 0xf, 0xf, true));
}
constexpr int XOR1 = 0xB1, XOR2 = 0x4E, XOR7 = 0x141, XOR8 = 0x128, ROR4 = 0x124;
__device__ __forceinline__ float xrow16_max(float x) {
  auto s = __builtin_amdgcn_permlane16_swap(__float_as_uint(x), __float_as_uint(x), false, false);
  x = fmaxf(__uint_as_float(s[0]), __uint_as_float(s[1]));
  auto t = __builtin_amdgcn_permlane32_swap(__float_as_uint(x), __float_as_uint(x), false, false);
  return fmaxf(__uint_as_float(t[0]), __uint_as_float(t[1]));
}
__device__ __forceinline__ float xrow16_sum(float x) {
  auto s = __builtin_amdgcn_permlane16_swap(__float_as_uint(x), __float_as_uint(x), false, false);
  x = __uint_as_float(s[0]) + __uint_as_float(s[1]);
  auto t = __builtin_amdgcn_permlane32_swap(__float_as_uint(x), __float_as_uint(x), false, false);
  return __uint_as_float(t[0]) + __uint_as_float(t[1]);
}
__device__ __forceinline__ float lo16(unsigned u) { return __builtin_bit_cast(float, u << 16); }
__device__ __forceinline__ float hi16(unsigned u) { return __builtin_bit_cast(float, u & 0xffff0000u); }
#define AUXW 2
#define STAMP(k)
typedef __amdgpu_buffer_rsrc_t rsrc_t;
__device__ __forceinline__ rsrc_t mkr(const void* p, int bytes) { return __builtin_amdgcn_make_buffer_rsrc((void*)p, 0, bytes, 0x00020000); }
template <int AUX> __device__ __forceinline__ v4u ldb(rsrc_t r, int voff, int soff) { return __builtin_amdgcn_raw_buffer_load_b128(r, voff, soff, AUX); }
#define SB() __builtin_amdgcn_sched_barrier(0)
#define BAR() do { asm volatile("s_waitcnt lgkmcnt(0)" ::: "memory"); __builtin_amdgcn_s_barrier(); asm volatile("" ::: "memory"); } while (0)

template <int J> struct BC { static constexpr int v = 0x150 + J; };

__global__ void __launch_bounds__(512, 2) k_attn(Params P) {
  __shared__ __attribute__((aligned(16))) float lds[8 * 128 + 8 * 128 + 64];
  float* qpart = lds;
  float* opart = lds + 1024;
  float* red = lds + 2048;
  const int tid = threadIdx.x, lane = tid & 63, w = __builtin_amdgcn_readfirstlane(tid >> 6);
  const int r = lane >> 4, c = lane & 15;
  const int bid = blockIdx.x, xcd = bid & 7, jj = bid >> 3, hp = xcd * 8 + (jj >> 2);
  const int br = hp >> 5, h = hp & 31, b = (jj >> 1) & 1, sp = jj & 1;

  STAMP(0);
  const rsrc_t xr = mkr(P.x[br] + b * DM, DM * 2);
  const rsrc_t wqr = mkr(P.wq[br] + (size_t)(64 * w) * HD + h * DH, 64 * HD * 2);
  const size_t kvoff = ((size_t)(b * NH + h) * SK + (SK - WIN) + sp * 1024 + w * 16) * DH;
  const rsrc_t kr = mkr(P.k[br] + kvoff, 1024 * 256);
  const rsrc_t vr = mkr(P.v[br] + kvoff, 1024 * 256);
  const int g = lane >> 3, c8 = lane & 7;
  const rsrc_t wor = mkr(P.wo[br] + (size_t)(h * DH) * DM + 64 * w, 128 * DM * 2);
  const int xo = (64 * w + 16 * r) * 2, qo = 16 * r * HD * 2 + c * 16, lo = lane * 16, oo = 16 * g * DM * 2 + c8 * 16;
#define KVOFF(n) (((n) >> 2) * 32768 + ((n) & 3) * 1024)
  v4u xa = ldb<0>(xr, xo, 0), xb = ldb<0>(xr, xo, 16);
  v4u A[16], K[32];
#pragma unroll
  for (int j = 0; j < 16; ++j) { A[j] = ldb<AUXW>(wqr, qo, j * HD * 2); SB(); }
#pragma unroll
  for (int j = 0; j < 32; ++j) { K[j] = ldb<2>(kr, lo, KVOFF(j)); SB(); }
  STAMP(1);
  float qa[8];
#pragma unroll
  for (int i = 0; i < 8; ++i) qa[i] = 0.f;
#pragma unroll
  for (int j = 0; j < 16; j += 2) {
    const unsigned xx = j < 8 ? xa[j / 2] : xb[j / 2 - 4];
#pragma unroll
    for (int k = 0; k < 4; ++k) {
      const unsigned lo = __builtin_amdgcn_perm(A[j + 1][k], A[j][k], 0x05040100u);
      const unsigned hi = __builtin_amdgcn_perm(A[j + 1][k], A[j][k], 0x07060302u);
      qa[2 * k] = dot2(lo, xx, qa[2 * k]);
      qa[2 * k + 1] = dot2(hi, xx, qa[2 * k + 1]);
    }
    SB();
    A[j] = ldb<2>(vr, lo, KVOFF(j));
    A[j + 1] = ldb<2>(vr, lo, KVOFF(j + 1));
    SB();
  }
#pragma unroll
  for (int i = 0; i < 8; ++i) qa[i] = xrow16_sum(qa[i]);
  if (r == 0) {
    *(f32x4*)(qpart + w * 128 + 8 * c) = f32x4{qa[0], qa[1], qa[2], qa[3]};
    *(f32x4*)(qpart + w * 128 + 8 * c + 4) = f32x4{qa[4], qa[5], qa[6], qa[7]};
  }
  STAMP(2);
  BAR();
  STAMP(3);
  f32x4 q0 = {0.f, 0.f, 0.f, 0.f}, q1 = {0.f, 0.f, 0.f, 0.f};
#pragma unroll
  for (int ww = 0; ww < 8; ++ww) {
    q0 += *(const f32x4*)(qpart + ww * 128 + 8 * c);
    q1 += *(const f32x4*)(qpart + ww * 128 + 8 * c + 4);
  }
  const v4u q = {cvtpk(q0[0], q0[1]), cvtpk(q0[2], q0[3]), cvtpk(q1[0], q1[1]), cvtpk(q1[2], q1[3])};
  float s[2];
#pragma unroll
  for (int hf = 0; hf < 2; ++hf) {
    float x[16];
#pragma unroll
    for (int j = 0; j < 16; ++j) {
      const int jj2 = hf * 16 + j;
      float d = 0.f;
#pragma unroll
      for (int k = 0; k < 4; ++k) d = dot2(K[jj2][k], q[k], d);
      x[j] = d;
      SB();
      if (hf == 0) K[jj2] = ldb<2>(vr, lo, KVOFF(16 + j));
      else K[jj2] = ldb<AUXW>(wor, oo, j * DM * 2);
      SB();
    }
#define TR_STEP(HALF, CTRL, BIT) _Pragma("unroll") for (int hh = 0; hh < (HALF); ++hh) { \
      const float lo_ = x[hh], hi_ = x[hh + (HALF)]; const float t1_ = lo_ + dpp<CTRL>(lo_), t2_ = hi_ + dpp<CTRL>(hi_); \
      x[hh] = (c & (BIT)) ? t2_ : t1_; }
    TR_STEP(8, XOR8, 8) TR_STEP(4, XOR7, 4) TR_STEP(2, XOR2, 2) TR_STEP(1, XOR1, 1)
#undef TR_STEP
    s[hf] = rbf(rbf(x[0]) * 0.08838834764831845f);
  }
  float m = fmaxf(s[0], s[1]);
  m = fmaxf(m, dpp<XOR1>(m)); m = fmaxf(m, dpp<XOR2>(m)); m = fmaxf(m, dpp<XOR7>(m)); m = fmaxf(m, dpp<XOR8>(m));
  m = xrow16_max(m);
  if (lane == 0) red[w] = m;
  STAMP(4);
  BAR();
  {
    const f32x4 m0 = *(const f32x4*)(red), m1 = *(const f32x4*)(red + 4);
    m = fmaxf(fmaxf(fmaxf(m0[0], m0[1]), fmaxf(m0[2], m0[3])), fmaxf(fmaxf(m1[0], m1[1]), fmaxf(m1[2], m1[3])));
  }
  constexpr float L2E = 1.4426950408889634f;
  float p[2];
  p[0] = __builtin_amdgcn_exp2f((s[0] - m) * L2E);
  p[1] = __builtin_amdgcn_exp2f((s[1] - m) * L2E);
  float l = p[0] + p[1];
  l += dpp<XOR1>(l); l += dpp<XOR2>(l); l += dpp<XOR7>(l); l += dpp<XOR8>(l);
  l = xrow16_sum(l);
  if (lane == 0) red[16 + w] = l;
  float acc[8];
#pragma unroll
  for (int i = 0; i < 8; ++i) acc[i] = 0.f;
#define PV(J, REG) { const float pj = dpp<0x150 + ((J) & 15)>(p[(J) >> 4]); \
    _Pragma("unroll") for (int k = 0; k < 4; ++k) { acc[2 * k] = fmaf(pj, lo16(REG[k]), acc[2 * k]); acc[2 * k + 1] = fmaf(pj, hi16(REG[k]), acc[2 * k + 1]); } SB(); }
  PV(0, A[0]) PV(1, A[1]) PV(2, A[2]) PV(3, A[3]) PV(4, A[4]) PV(5, A[5]) PV(6, A[6]) PV(7, A[7])
  PV(8, A[8]) PV(9, A[9]) PV(10, A[10]) PV(11, A[11]) PV(12, A[12]) PV(13, A[13]) PV(14, A[14]) PV(15, A[15])
  PV(16, K[0]) PV(17, K[1]) PV(18, K[2]) PV(19, K[3]) PV(20, K[4]) PV(21, K[5]) PV(22, K[6]) PV(23, K[7])
  PV(24, K[8]) PV(25, K[9]) PV(26, K[10]) PV(27, K[11]) PV(28, K[12]) PV(29, K[13]) PV(30, K[14]) PV(31, K[15])
#undef PV
#pragma unroll
  for (int i = 0; i < 8; ++i) acc[i] = xrow16_sum(acc[i]);
  if (r == 0) {
    *(f32x4*)(opart + w * 128 + 8 * c) = f32x4{acc[0], acc[1], acc[2], acc[3]};
    *(f32x4*)(opart + w * 128 + 8 * c + 4) = f32x4{acc[4], acc[5], acc[6], acc[7]};
  }
  STAMP(5);
  BAR();
  STAMP(6);
  float o2[2] = {0.f, 0.f};
#pragma unroll
  for (int ww = 0; ww < 8; ++ww) {
    const float2 t = *(const float2*)(opart + ww * 128 + 2 * lane);
    o2[0] += t.x; o2[1] += t.y;
  }
  float y[8];
#pragma unroll
  for (int i = 0; i < 8; ++i) y[i] = 0.f;
#pragma unroll
  for (int j = 0; j < 16; ++j) {
    const float oj = __shfl(o2[j & 1], (lane & ~7) + (j >> 1), 64);
#pragma unroll
    for (int k = 0; k < 4; ++k) {
      y[2 * k] = fmaf(oj, lo16(K[16 + j][k]), y[2 * k]);
      y[2 * k + 1] = fmaf(oj, hi16(K[16 + j][k]), y[2 * k + 1]);
    }
  }
#pragma unroll
  for (int i = 0; i < 8; ++i) { y[i] += dpp<XOR8>(y[i]); y[i] = xrow16_sum(y[i]); }
  const int grp = br * 2 + b, pidx = h * 2 + sp;
  float* yp = P.ws + 1024 + (size_t)(grp * 64 + pidx) * 512;
  if (lane < 8) {
    *(f32x4*)(yp + 64 * w + 8 * c8) = f32x4{y[0], y[1], y[2], y[3]};
    *(f32x4*)(yp + 64 * w + 8 * c8 + 4) = f32x4{y[4], y[5], y[6], y[7]};
  }
  if (tid == 0) {
    float L = 0.f;
#pragma unroll
    for (int ww = 0; ww < 8; ++ww) L += red[16 + ww];
    *(float2*)(P.ws + (grp * 64 + pidx) * 2) = make_float2(m, L);
  }
  STAMP(7);
}

__global__ void __launch_bounds__(64) k_comb(const float* ws, unsigned short* out) {
  const int lane = threadIdx.x, g = lane >> 3, c = lane & 7;
  const int grp = blockIdx.x >> 4, cb = blockIdx.x & 15;
  f32x4 ml[8], yv[8];
#pragma unroll
  for (int i = 0; i < 8; ++i) {
    const int p = 8 * i + g;
    ml[i] = *(const f32x4*)(ws + (grp * 64 + (p & ~1)) * 2);
    yv[i] = *(const f32x4*)(ws + 1024 + (size_t)(grp * 64 + p) * 512 + cb * 32 + 4 * c);
  }
  constexpr float L2E = 1.4426950408889634f;
  f32x4 a = {0.f, 0.f, 0.f, 0.f};
#pragma unroll
  for (int i = 0; i < 8; ++i) {
    const float M = fmaxf(ml[i][0], ml[i][2]);
    const float e0 = __builtin_amdgcn_exp2f((ml[i][0] - M) * L2E), e1 = __builtin_amdgcn_exp2f((ml[i][2] - M) * L2E);
    const float L = e0 * ml[i][1] + e1 * ml[i][3];
    const float wt = ((g & 1) ? e1 : e0) / L;
    a += wt * yv[i];
  }
#pragma unroll
  for (int i = 0; i < 4; ++i) { a[i] += dpp<XOR8>(a[i]); a[i] = xrow16_sum(a[i]); }
  if (lane < 8) {
    const int br = grp >> 1, b = grp & 1;
    v2u o = {cvtpk(a[0], a[1]), cvtpk(a[2], a[3])};
    *(v2u*)(out + b * 1024 + br * 512 + cb * 32 + 4 * c) = o;
  }
}

extern "C" void kernel_launch(void* const* d_in, const int* in_sizes, int n_in,
                              void* d_out, int out_size, void* d_ws, size_t ws_size,
                              hipStream_t stream) {
  Params P;
  for (int i = 0; i < 2; ++i) {
    P.x[i] = (const unsigned short*)d_in[5 * i + 0];
    P.k[i] = (const unsigned short*)d_in[5 * i + 1];
    P.v[i] = (const unsigned short*)d_in[5 * i + 2];
    P.wq[i] = (const unsigned short*)d_in[5 * i + 3];
    P.wo[i] = (const unsigned short*)d_in[5 * i + 4];
  }
  P.out = (unsigned short*)d_out;
  P.ws = (float*)d_ws;
  hipLaunchKernelGGL(k_attn, dim3(256), dim3(512), 0, stream, P);
  hipLaunchKernelGGL(k_comb, dim3(64), dim3(64), 0, stream, (const float*)d_ws, (unsigned short*)d_out);
}
```

```cpp
#include <hip/hip_runtime.h>
#include <hip/hip_bf16.h>
#include <stdint.h>

typedef unsigned v4u __attribute__((ext_vector_type(4)));
typedef unsigned v2u __attribute__((ext_vector_type(2)));
typedef unsigned v8u __attribute__((ext_vector_type(8)));
typedef float f32x4 __attribute__((ext_vector_type(4)));
typedef float f32x2 __attribute__((ext_vector_type(2)));
typedef __bf16 bf16x2 __attribute__((ext_vector_type(2)));
typedef __bf16 bf16x8 __attribute__((ext_vector_type(8)));
typedef __bf16 bf16x16 __attribute__((ext_vector_type(16)));
typedef unsigned long long u64x8 __attribute__((ext_vector_type(8)));
typedef unsigned long long u64x4 __attribute__((ext_vector_type(4)));
typedef __amdgpu_buffer_rsrc_t rsrc_t;
#define LAS __attribute__((address_space(3)))
#define CAS __attribute__((address_space(4)))

constexpr int SK = 8192, WIN = 2048, DH = 128, NH = 32, DM = 512, HD = 4096;

#define DINIT 3
#define RATE 244
#define OFFS 100
#define RATEB 286
#define OFFSB 450
#define XR4 16
#define GAP 200
#define QRD 27
#define WO0 23
#define FRD 50
#define TSYNC 8
#define PMIN 12
#define PLEAD 135
#define PLMEAN 200
constexpr int DMAX = 5;
constexpr int SMAX = (31 + DMAX > QRD + 7) ? 31 + DMAX : QRD + 7;
constexpr int clampi(int v, int lo, int hi) { return v < lo ? lo : v > hi ? hi : v; }
constexpr int wq_lo(int n) { return clampi(n, 0, 16); }
constexpr int wo_lo(int n) { return clampi(n - WO0, 0, 32); }
constexpr int extras(int n) {
  return n < DINIT - 1 ? 0 : n == DINIT - 1 ? wq_lo(DINIT) + 4 : wo_lo(n + 1) - wo_lo(n) + wq_lo(n + 1) - wq_lo(n) + (n == TSYNC);
}
constexpr int pace(int n, int d) { if (d > n) return 63; int c = d - 1; for (int i = n - d; i < n; ++i) c += extras(i); return c; }
constexpr int units8(int n) { return 8 * n + 2 * wq_lo(n) + wo_lo(n); }
constexpr int dith(int n) { constexpr int t[4] = {0, 2, 1, 3}; return t[n & 3]; }
constexpr int thr(int n, int d) {
  const int a = OFFS + (units8(n) * RATE) / 8, b = OFFSB + (units8(n) * RATEB) / 8, e = (units8(n) * XR4) / 8;
  return ((d == 3 ? a + ((b - a) * dith(n)) / 3 : d == 4 ? b + (e * dith(n)) / 3 : b + e) + (d - 3) * GAP) / 10;
}
constexpr int wq_pairs(int n) { return wq_lo(n - DMAX) / 2; }
constexpr int wo_pairs(int n) { return wo_lo(n - DMAX) / 2; }
constexpr int QPUB = 16 + DMAX;
constexpr int pvdone(int n) {
  int d = 0;
  for (int i = SMAX + 1; i <= n; ++i) d = clampi(i - DMAX - 31, 0, d + 2);
  return d;
}
static_assert(QPUB < QRD && QRD + 3 <= SMAX - 3 && wo_pairs(63) == 16 && SMAX < FRD && FRD < 62 && WO0 >= DINIT && TSYNC >= DINIT, "schedule");

constexpr int WS_EPOCH = 0;
constexpr int WS_T = 2048;
constexpr int WS_L = 4096;
constexpr int WS_Y = 8192;
constexpr int WS_END = WS_Y + 256 * 2048;
constexpr unsigned SPIN_CAP = 1u << 18;
constexpr int OOB = 0x7fffff00;

struct Params {
  const unsigned short* k[2];
  const unsigned short* wq[2];
  const unsigned short* x[2];
  unsigned char* ws;
  unsigned short* out;
  const unsigned short* v[2];
  const unsigned short* wo[2];
};

__device__ __forceinline__ float dot2(unsigned a, unsigned b, float acc) {
  return __builtin_amdgcn_fdot2_f32_bf16(__builtin_bit_cast(bf16x2, a), __builtin_bit_cast(bf16x2, b), acc, false);
}
__device__ __forceinline__ unsigned cvtpk(float lo, float hi) {
  bf16x2 r; r.x = (__bf16)lo; r.y = (__bf16)hi; return __builtin_bit_cast(unsigned, r);
}
__device__ __forceinline__ float rbf(float x) { return (float)(__bf16)x; }
template <int CTRL> __device__ __forceinline__ float dpp(float x) {
  return __builtin_bit_cast(float, __builtin_amdgcn_mov_dpp(__builtin_bit_cast(int, x), CTRL, 0xf, 0xf, true));
}
template <int CTRL> __device__ __forceinline__ int dppi(int x) { return __builtin_amdgcn_mov_dpp(x, CTRL, 0xf, 0xf, true); }
constexpr int XOR1 = 0xB1, XOR2 = 0x4E, XOR7 = 0x141, XOR8 = 0x128, ROR4 = 0x124;
__device__ __forceinline__ float tr32(float a, float b) {
  auto t = __builtin_amdgcn_permlane32_swap(__float_as_uint(a), __float_as_uint(b), false, false);
  return __uint_as_float(t[0]) + __uint_as_float(t[1]);
}
__device__ __forceinline__ float tr16(float a, float b) {
  auto t = __builtin_amdgcn_permlane16_swap(__float_as_uint(a), __float_as_uint(b), false, false);
  return __uint_as_float(t[0]) + __uint_as_float(t[1]);
}
__device__ __forceinline__ float xrow16_max(float x) {
  auto s = __builtin_amdgcn_permlane16_swap(__float_as_uint(x), __float_as_uint(x), false, false);
  x = fmaxf(__uint_as_float(s[0]), __uint_as_float(s[1]));
  auto t = __builtin_amdgcn_permlane32_swap(__float_as_uint(x), __float_as_uint(x), false, false);
  return fmaxf(__uint_as_float(t[0]), __uint_as_float(t[1]));
}
__device__ __forceinline__ float lo16(unsigned u) { return __builtin_bit_cast(float, u << 16); }
__device__ __forceinline__ float hi16(unsigned u) { return __builtin_bit_cast(float, u & 0xffff0000u); }
__device__ __forceinline__ rsrc_t mkr(const void* p, int bytes) { return __builtin_amdgcn_make_buffer_rsrc((void*)p, 0, bytes, 0x00020000); }
template <int AUX> __device__ __forceinline__ v4u ldb(rsrc_t r, int voff, int soff) { return __builtin_amdgcn_raw_buffer_load_b128(r, voff, soff, AUX); }
template <int AUX> __device__ __forceinline__ v2u ldb2(rsrc_t r, int voff, int soff) { return __builtin_amdgcn_raw_buffer_load_b64(r, voff, soff, AUX); }
constexpr int NT = 2, SC1 = 16;
__device__ __forceinline__ unsigned gpack(float v, unsigned tb) { return ((__float_as_uint(v) + 0x80u) & 0xffffff00u) | tb; }
__device__ __forceinline__ float gval(unsigned g) { return __uint_as_float(g & 0xffffff00u); }
__device__ __forceinline__ unsigned binc(unsigned e) { return ((e & 0x7f7f7f7fu) + 0x01010101u) ^ (e & 0x80808080u); }
__device__ __forceinline__ unsigned now() { return (unsigned)__builtin_amdgcn_s_memrealtime(); }

#define SB() __builtin_amdgcn_sched_barrier(0)
#define MEMFENCE() asm volatile("" ::: "memory")
#define PIN(x) asm volatile("" : "+v"(x))
#define BAR() do { SB(); asm volatile("s_waitcnt lgkmcnt(0)" ::: "memory"); __builtin_amdgcn_s_barrier(); asm volatile("" ::: "memory"); SB(); } while (0)
template <int N> struct IC { static constexpr int v = N; };
template <int LO, int HI, class F> __device__ __forceinline__ void sfor(F&& f) { if constexpr (LO < HI) { f(IC<LO>{}); sfor<LO + 1, HI>(f); } }
template <int N> __device__ __forceinline__ unsigned pacestep(unsigned t0) {
  unsigned d;
  asm volatile("s_waitcnt vmcnt(%2)\n\ts_memrealtime vcc\n\ts_waitcnt lgkmcnt(0)\n\ts_sub_i32 %0, vcc_lo, %1\n\t"
               "s_cmpk_ge_i32 %0, %6\n\ts_cbranch_scc1 1f\n\ts_waitcnt vmcnt(%3)\n\t"
               "s_cmpk_ge_i32 %0, %7\n\ts_cbranch_scc1 1f\n\ts_waitcnt vmcnt(%4)\n\t"
               "s_cmpk_ge_i32 %0, %8\n\ts_cbranch_scc1 1f\n\ts_waitcnt vmcnt(%5)\n1:"
               : "=s"(d)
               : "s"(t0), "n"(pace(N, 5)), "n"(pace(N, 4)), "n"(pace(N, 3)), "n"(pace(N, 2)), "n"(thr(N, 5)), "n"(thr(N, 4)), "n"(thr(N, 3))
               : "scc", "vcc");
  SB();
  return d;
}
#define STAMP(k)
#define STAMPOUT(slot)

constexpr int OPS = 36;
constexpr int L_QP = 0;
constexpr int L_OP = 1024;
constexpr int L_ML = L_OP + 16 * OPS;
constexpr int L_FLG = L_ML + 16;
constexpr int L_END = L_FLG + 16;
constexpr int POLLW = 7;

__global__ void __launch_bounds__(512, 2) __attribute__((aligned(4096))) k_fused(Params P) {
  __shared__ __attribute__((aligned(16))) float lds[L_END];
  volatile LAS unsigned* flg = (volatile LAS unsigned*)(lds + L_FLG);
  const u64x8 ka = *(const CAS u64x8*)__builtin_amdgcn_kernarg_segment_ptr();
  SB();
  STAMP(0);
  const int tid = threadIdx.x, lane = tid & 63, w = __builtin_amdgcn_readfirstlane(tid >> 6);
  const int r = lane >> 4, c = lane & 15;
  const int bid = blockIdx.x, xcd = bid & 7, jj = bid >> 3, hp = xcd * 8 + (jj >> 2);
  const int br = hp >> 5, h = hp & 31, b = (jj >> 1) & 1, sp = jj & 1;
  const int grp = br * 2 + b, pidx = h * 2 + sp;
  constexpr float L2E = 1.4426950408889634f;
  unsigned kvoff = (((b * NH + h) * SK + (SK - WIN) + sp * 1024 + w * 16) * DH) * 2;
  unsigned wqoff = (64 * w * HD + h * DH) * 2, xoff = (b * DM + 64 * w) * 2;
  int lo = lane * 16;
  int qo = 16 * r * (HD * 2) + c * 16;
  const int oo = 8 * r * (DM * 2) + c * 8;
  if (lane < 2) flg[lane * 8 + w] = 0u;
  asm volatile("" : "+s"(kvoff), "+s"(wqoff), "+s"(xoff));
  asm volatile("" : "+v"(lo), "+v"(qo));
  SB();
  const int kvbytes = 1024 * 256 - w * 4096;
  const rsrc_t kr = mkr((const void*)((br ? ka[1] : ka[0]) + kvoff), kvbytes);
  rsrc_t wqr, vr, wor;
#define KVOFF(n) (((n) >> 2) * 32768 + ((n) & 3) * 1024)
#define LDK(n) ldb<NT>(kr, lo, KVOFF(n))
#define LDV(n) ldb<NT>(vr, lo, KVOFF(n))
#define LDQ(n) ldb<NT>(wqr, qo, (n) * (HD * 2))
#define LDO(n) ldb2<NT>(wor, oo, (((n) >> 3) * 32 + ((n) & 7)) * (DM * 2))

  v4u A[16], K[32], V[32];
  v2u O[32];
  asm volatile("" :: "s"(kr));
  SB();
  STAMP(1);
  const unsigned long long t0l = __builtin_amdgcn_s_memrealtime();
  SB();
  sfor<0, DINIT>([&](auto I) { K[I.v] = LDK(I.v); SB(); });
  wqr = mkr((const void*)((br ? ka[3] : ka[2]) + wqoff), 63 * HD * 2 + DH * 2);
  sfor<0, wq_lo(DINIT)>([&](auto J) { A[J.v] = LDQ(J.v); SB(); });
  const rsrc_t xr = mkr((const void*)((br ? ka[5] : ka[4]) + xoff), 128);
  const rsrc_t wsr = mkr((const void*)ka[6], WS_END);
  const v4u xa = ldb<NT>(xr, 32 * r, 0), xb = ldb<NT>(xr, 32 * r, 16);
  const v2u epv = ldb2<SC1 | NT>(wsr, 0, WS_EPOCH + 256 * grp);
  BAR();
  asm volatile("" :: "s"(t0l));
  unsigned t0 = (unsigned)t0l, tsv;
  __builtin_amdgcn_raw_buffer_store_b32(t0, wsr, (tid == 0 && bid < 8) ? 0 : OOB, WS_T + 4 * xcd, SC1);
  {
    const u64x4 kb = *((const CAS u64x4*)__builtin_amdgcn_kernarg_segment_ptr() + 2);
    vr = mkr((const void*)((br ? kb[1] : kb[0]) + kvoff), kvbytes);
    wor = mkr((const void*)((br ? kb[3] : kb[2]) + (h * DH * DM + 64 * w) * 2), 127 * DM * 2 + 128);
  }

  v4u q;
  float qa[8], x[16], s[2], p[2], acc[8], m, lam;
  v8u WB[4][2];
  unsigned tg0, tg1;
  LAS unsigned* wrp; const LAS v4u* rdp; int yso, ysb, glo, sidx; unsigned tgb;
  unsigned short* outp = (unsigned short*)ka[7] + b * 1024 + br * 512 + pidx * 8;
#pragma unroll
  for (int i = 0; i < 8; ++i) acc[i] = qa[i] = 0.f;

  auto QP = [&](auto I) {
    constexpr int i = I.v;
    const unsigned xx = i < 4 ? xa[i & 3] : xb[i & 3];
#pragma unroll
    for (int k = 0; k < 4; ++k) {
      qa[2 * k] = dot2(__builtin_amdgcn_perm(A[2 * i + 1][k], A[2 * i][k], 0x05040100u), xx, qa[2 * k]);
      qa[2 * k + 1] = dot2(__builtin_amdgcn_perm(A[2 * i + 1][k], A[2 * i][k], 0x07060302u), xx, qa[2 * k + 1]);
    }
#pragma unroll
    for (int k = 0; k < 8; ++k) PIN(qa[k]);
  };
  auto QK = [&](auto I) {
    constexpr int n = I.v;
    float d = 0.f;
#pragma unroll
    for (int k = 0; k < 4; ++k) d = dot2(K[n][k], q[k], d);
    x[n & 15] = d;
    PIN(x[n & 15]);
  };
  auto TR = [&](int hf) {
#define TR_STEP(HALF, CTRL, BIT) _Pragma("unroll") for (int hh = 0; hh < (HALF); ++hh) { \
      const float lo_ = x[hh], hi_ = x[hh + (HALF)]; const float t1_ = lo_ + dpp<CTRL>(lo_), t2_ = hi_ + dpp<CTRL>(hi_); \
      x[hh] = (c & (BIT)) ? t2_ : t1_; }
    TR_STEP(8, XOR8, 8) TR_STEP(4, XOR7, 4) TR_STEP(2, XOR2, 2) TR_STEP(1, XOR1, 1)
#undef TR_STEP
    s[hf] = rbf(rbf(x[0]) * 0.08838834764831845f);
    PIN(s[hf]);
  };
  auto PV = [&](auto I) {
    constexpr int j = I.v;
    const float pj = dpp<0x150 + (j & 15)>(p[j >> 4]);
#pragma unroll
    for (int k = 0; k < 4; ++k) { acc[2 * k] = fmaf(pj, lo16(V[j][k]), acc[2 * k]); acc[2 * k + 1] = fmaf(pj, hi16(V[j][k]), acc[2 * k + 1]); }
#pragma unroll
    for (int k = 0; k < 8; ++k) PIN(acc[k]);
    SB();
  };
  auto WP = [&](auto I) {
    constexpr int i = I.v;
    WB[0][i >> 3][i & 7] = __builtin_amdgcn_perm(O[2 * i + 1][0], O[2 * i][0], 0x05040100u);
    WB[1][i >> 3][i & 7] = __builtin_amdgcn_perm(O[2 * i + 1][0], O[2 * i][0], 0x07060302u);
    WB[2][i >> 3][i & 7] = __builtin_amdgcn_perm(O[2 * i + 1][1], O[2 * i][1], 0x05040100u);
    WB[3][i >> 3][i & 7] = __builtin_amdgcn_perm(O[2 * i + 1][1], O[2 * i][1], 0x07060302u);
    if constexpr ((i & 7) == 7) { PIN(WB[0][i >> 3]); PIN(WB[1][i >> 3]); PIN(WB[2][i >> 3]); PIN(WB[3][i >> 3]); }
  };

  sfor<DINIT, 64>([&](auto I) {
    constexpr int n = I.v;
    const unsigned tn = pacestep<n>(t0);
    if constexpr (n < 32) K[n] = LDK(n); else V[n - 32] = LDV(n - 32);
    if constexpr (n < 16) A[n] = LDQ(n);
    if constexpr (n >= WO0 && n < WO0 + 32) O[n - WO0] = LDO(n - WO0);
    if constexpr (n == TSYNC) tsv = __builtin_amdgcn_raw_buffer_load_b32(wsr, (lane & 7) * 4, WS_T, SC1);
    if constexpr (n == 63)
      __builtin_amdgcn_raw_buffer_store_b128(v4u{__float_as_uint(lam), tg0, tn + t0, tg1}, wsr, tid ? OOB : 0, WS_L + (grp * 64 + pidx) * 16, SC1);
    SB();
    if constexpr (n == TSYNC + DMAX + 1) {
      int d = (int)(t0 - tsv);
      d = (unsigned)d < 400u ? d : 0;
      d = max(d, dppi<XOR1>(d)); d = max(d, dppi<XOR2>(d)); d = max(d, dppi<XOR7>(d));
      t0 -= __builtin_amdgcn_readfirstlane(d);
      asm volatile("" : "+s"(t0));
    }
    if constexpr (n <= QPUB) sfor<wq_pairs(n - 1), wq_pairs(n)>(QP);
    if constexpr (n == QPUB) {
      float t[4];
#pragma unroll
      for (int i = 0; i < 4; ++i) t[i] = tr32(qa[i], qa[4 + i]);
      f32x2 u;
#pragma unroll
      for (int i = 0; i < 2; ++i) u[i] = tr16(t[i], t[2 + i]);
      *(f32x2*)(lds + L_QP + w * 128 + 8 * c + 2 * r) = u;
      MEMFENCE();
      if (lane == 0) flg[w] = 1u;
      tg0 = binc(__builtin_amdgcn_readfirstlane(epv[0])); tg1 = binc(__builtin_amdgcn_readfirstlane(epv[1]));
      asm volatile("" : "+s"(outp), "+s"(tg0), "+s"(tg1));
    }
    if constexpr (n == QRD) {
      STAMP(2);
      while (!__all(flg[lane & 7] != 0u)) __builtin_amdgcn_s_sleep(1);
      MEMFENCE();
      f32x4 q0 = {0.f, 0.f, 0.f, 0.f}, q1 = {0.f, 0.f, 0.f, 0.f};
#pragma unroll
      for (int ww = 0; ww < 8; ++ww) {
        q0 += *(const f32x4*)(lds + L_QP + ww * 128 + 8 * c);
        q1 += *(const f32x4*)(lds + L_QP + ww * 128 + 8 * c + 4);
      }
      q = v4u{cvtpk(q0[0], q0[1]), cvtpk(q0[2], q0[3]), cvtpk(q1[0], q1[1]), cvtpk(q1[2], q1[3])};
      PIN(q);
    }
    if constexpr (n > QRD && n <= QRD + 2) sfor<8 * (n - QRD - 1), 8 * (n - QRD - 1) + 8>(QK);
    if constexpr (n == QRD + 3) TR(0);
    if constexpr (n >= SMAX - 3 && n <= SMAX) sfor<16 + 4 * (n - (SMAX - 3)), 20 + 4 * (n - (SMAX - 3))>(QK);
    if constexpr (n == SMAX) {
      TR(1);
      m = fmaxf(s[0], s[1]);
      m = fmaxf(m, dpp<XOR1>(m)); m = fmaxf(m, dpp<XOR2>(m)); m = fmaxf(m, dpp<XOR7>(m)); m = fmaxf(m, dpp<XOR8>(m));
      m = xrow16_max(m);
      p[0] = __builtin_amdgcn_exp2f((s[0] - m) * L2E);
      p[1] = __builtin_amdgcn_exp2f((s[1] - m) * L2E);
      float l = p[0] + p[1];
      l += dpp<XOR1>(l); l += dpp<XOR2>(l); l += dpp<XOR7>(l); l += dpp<XOR8>(l);
      l = tr16(l, l); l = tr32(l, l);
      if (lane == 0) { *(f32x2*)(lds + L_ML + 2 * w) = f32x2{m, l}; MEMFENCE(); flg[8 + w] = 1u; }
      STAMP(3);
    }
    if constexpr (n == FRD) {
      while (!__all(flg[8 + (lane & 7)] != 0u)) __builtin_amdgcn_s_sleep(1);
      MEMFENCE();
      const f32x2 ml = *(const f32x2*)(lds + L_ML + 2 * (lane & 7));
      float M = ml[0];
      M = fmaxf(M, dpp<XOR1>(M)); M = fmaxf(M, dpp<XOR2>(M)); M = fmaxf(M, dpp<XOR7>(M));
      float L = ml[1] * __builtin_amdgcn_exp2f((ml[0] - M) * L2E);
      L += dpp<XOR1>(L); L += dpp<XOR2>(L); L += dpp<XOR7>(L);
      const float f = __builtin_amdgcn_exp2f((m - M) * L2E) / L;
      p[0] *= f; p[1] *= f;
#pragma unroll
      for (int k = 0; k < 8; ++k) { acc[k] *= f; PIN(acc[k]); }
      lam = fmaf(M, L2E, __builtin_amdgcn_logf(L));
      PIN(lam); PIN(p[0]); PIN(p[1]);
    }
    sfor<wo_pairs(n - 1), wo_pairs(n)>(WP);
    if constexpr (n == 62) {
      wrp = (LAS unsigned*)lds + L_OP + (w + 8 * (r & 1)) * OPS + 2 * c + (r >> 1);
      rdp = (const LAS v4u*)((LAS unsigned*)lds + L_OP + c * OPS + 4 * r);
      sidx = (c & 8) ? 0xEEEE : 0x4444;
      yso = (4 * c + r) * 4;
      ysb = WS_Y + (grp * 64 + pidx) * 2048 + 256 * w;
      tgb = (((c & 1) ? tg1 : tg0) >> (8 * r)) & 0xffu;
      glo = w == POLLW ? lo : OOB;
      asm volatile("" : "+v"(wrp), "+v"(rdp), "+v"(yso), "+v"(tgb), "+v"(glo), "+v"(sidx));
      asm volatile("" : "+s"(ysb));
    }
    SB();
    if constexpr (n > SMAX) sfor<pvdone(n - 1), pvdone(n)>(PV);
    SB();
  });
  f32x4 d[4];
#pragma unroll
  for (int t = 0; t < 4; ++t) { d[t] = f32x4{0.f, 0.f, 0.f, 0.f}; PIN(d[t]); }
  sfor<pvdone(63), 32>(PV);
  STAMP(4);
  {
    float t[4];
#pragma unroll
    for (int i = 0; i < 4; ++i) t[i] = tr32(acc[i], acc[4 + i]);
    *wrp = cvtpk(tr16(t[0], t[2]), tr16(t[1], t[3]));
  }
  SB();
  v4u gl = ldb<SC1>(wsr, glo, WS_L + grp * 1024);
  BAR();
  STAMP(5);
  {
    const v4u af0 = rdp[0], af1 = rdp[4];
    SB();
#pragma unroll
    for (int t = 0; t < 4; ++t) d[t] = __builtin_amdgcn_smfmac_f32_16x16x64_bf16(__builtin_bit_cast(bf16x8, af0), __builtin_bit_cast(bf16x16, WB[t][0]), d[t], sidx, 0, 0);
    SB();
#pragma unroll
    for (int t = 0; t < 4; ++t) d[t] = __builtin_amdgcn_smfmac_f32_16x16x64_bf16(__builtin_bit_cast(bf16x8, af1), __builtin_bit_cast(bf16x16, WB[t][1]), d[t], sidx, 0, 0);
    SB();
    float e[4];
#pragma unroll
    for (int t = 0; t < 4; ++t) { const f32x2 h2 = f32x2{d[t][0], d[t][1]} + f32x2{d[t][2], d[t][3]}; e[t] = h2[0] + h2[1]; }
    const float y = tr16(tr32(e[0], e[2]), tr32(e[1], e[3]));
    __builtin_amdgcn_raw_buffer_store_b32(gpack(y, tgb), wsr, yso, ysb, SC1);
  }
  STAMP(6);
  if (w == POLLW) {
    const unsigned ts = now();
    unsigned spins = 0;
    if (__builtin_amdgcn_ballot_w64(((gl[1] ^ tg0) | (gl[3] ^ tg1)) != 0u) != 0ull) {
#pragma nounroll
      do {
        MEMFENCE();
        gl = ldb<SC1>(wsr, lo, WS_L + grp * 1024);
      } while (__builtin_amdgcn_ballot_w64(((gl[1] ^ tg0) | (gl[3] ^ tg1)) != 0u) != 0ull && ++spins < SPIN_CAP);
    }
    const float la = __uint_as_float(gl[0]);
    const float wl = __builtin_amdgcn_rcpf(1.0f + __builtin_amdgcn_exp2f(dpp<XOR1>(la) - la));
    float wt[2];
#pragma unroll
    for (int i = 0; i < 2; ++i) wt[i] = __int_as_float(__builtin_amdgcn_ds_bpermute(((lane >> 1) + 32 * i) * 4, __float_as_int(wl)));
    int dl = (int)(gl[2] - ts), dm = dl;
    dl = max(dl, dppi<XOR1>(dl)); dl = max(dl, dppi<XOR2>(dl)); dl = max(dl, dppi<XOR7>(dl)); dl = max(dl, dppi<XOR8>(dl));
    dl = max(max(__builtin_amdgcn_readlane(dl, 0), __builtin_amdgcn_readlane(dl, 16)), max(__builtin_amdgcn_readlane(dl, 32), __builtin_amdgcn_readlane(dl, 48)));
    dm += dppi<XOR1>(dm); dm += dppi<XOR2>(dm); dm += dppi<XOR7>(dm); dm += dppi<XOR8>(dm);
    dm = ((__builtin_amdgcn_readlane(dm, 0) + __builtin_amdgcn_readlane(dm, 16)) + (__builtin_amdgcn_readlane(dm, 32) + __builtin_amdgcn_readlane(dm, 48))) >> 6;
    dl = min(max(max(dl + PLEAD, dm + PLMEAN), PMIN), 400);
    int yo = (lane >> 1) * 2048 + (lane & 1) * 16, yb = WS_Y + grp * 64 * 2048 + pidx * 32, yb1 = yb + 32 * 2048;
    unsigned want = (lane & 1) ? tg1 : tg0;
    asm volatile("" : "+s"(yb), "+s"(yb1));
    const rsrc_t outr = mkr(outp, 16);
    int oso = (lane & 14) ? OOB : (lane & 1) * 8 + (lane >> 4) * 2;
    asm volatile("" : "+v"(yo), "+v"(want), "+v"(oso), "+v"(wt[0]), "+v"(wt[1]));
    while ((int)(now() - ts) < dl) {}
    v4u gy[2];
#pragma nounroll
    for (;;) {
      MEMFENCE();
      gy[0] = ldb<SC1>(wsr, yo, yb); gy[1] = ldb<SC1>(wsr, yo, yb1);
      unsigned bad = 0;
#pragma unroll
      for (int i = 0; i < 2; ++i) bad |= (__builtin_amdgcn_perm(gy[i][1], gy[i][0], 0x0c0c0400u) | __builtin_amdgcn_perm(gy[i][3], gy[i][2], 0x04000c0cu)) ^ want;
      if (__builtin_amdgcn_ballot_w64(bad != 0u) == 0ull) break;
      if (++spins >= SPIN_CAP) break;
    }
    float a[4];
#pragma unroll
    for (int k = 0; k < 4; ++k) a[k] = fmaf(wt[1], gval(gy[1][k]), wt[0] * gval(gy[0][k]));
    float z = tr16(tr32(a[0], a[2]), tr32(a[1], a[3]));
    z += dpp<XOR2>(z); z += dpp<ROR4>(z); z += dpp<XOR8>(z);
    __builtin_amdgcn_raw_buffer_store_b16((unsigned short)cvtpk(z, z), outr, oso, 0, NT);
    if (lane == 0 && pidx == 0) __builtin_amdgcn_raw_buffer_store_b64(v2u{tg0, tg1}, wsr, 0, WS_EPOCH + 256 * grp, SC1);
  }
  STAMP(7);
  STAMPOUT(bid * 8 + w);
}

extern "C" void kernel_launch(void* const* d_in, const int* in_sizes, int n_in,
                              void* d_out, int out_size, void* d_ws, size_t ws_size,
                              hipStream_t stream) {
  Params P;
  for (int i = 0; i < 2; ++i) {
    P.x[i] = (const unsigned short*)d_in[5 * i + 0];
    P.k[i] = (const unsigned short*)d_in[5 * i + 1];
    P.v[i] = (const unsigned short*)d_in[5 * i + 2];
    P.wq[i] = (const unsigned short*)d_in[5 * i + 3];
    P.wo[i] = (const unsigned short*)d_in[5 * i + 4];
  }
  P.out = (unsigned short*)d_out;
  P.ws = (unsigned char*)d_ws;
  hipLaunchKernelGGL(k_fused, dim3(256), dim3(512), 0, stream, P);
}
```

```cpp
#include <hip/hip_runtime.h>
#include <hip/hip_bf16.h>
#include <stdint.h>

typedef unsigned v4u __attribute__((ext_vector_type(4)));
typedef unsigned v2u __attribute__((ext_vector_type(2)));
typedef unsigned v8u __attribute__((ext_vector_type(8)));
typedef float f32x4 __attribute__((ext_vector_type(4)));
typedef float f32x2 __attribute__((ext_vector_type(2)));
typedef __bf16 bf16x2 __attribute__((ext_vector_type(2)));
typedef __bf16 bf16x8 __attribute__((ext_vector_type(8)));
typedef __bf16 bf16x16 __attribute__((ext_vector_type(16)));
typedef unsigned long long u64x8 __attribute__((ext_vector_type(8)));
typedef unsigned long long u64x4 __attribute__((ext_vector_type(4)));
typedef __amdgpu_buffer_rsrc_t rsrc_t;
#define LAS __attribute__((address_space(3)))
#define CAS __attribute__((address_space(4)))

constexpr int SK = 8192, WIN = 2048, DH = 128, NH = 32, DM = 512, HD = 4096;

#define DINIT 4
#define RATE 240
#define OFFS -220
#define RATEB 282
#define OFFSB 130
#define XR4 22
#define GAP 120
#define QPUB 10
#define QLD 22
#define QRD 30
#define WO0 5
#define WON 3
#define WOD 2
#define FRD 54
#define QSA (SC1 | NT)
#define QLA (SC1 | NT)
#define PMIN 12
#define PLEAD 125
#define PLMEAN 187
constexpr int DMAX = 5;
constexpr int K0 = 4, V0 = 36, NSTEP = 68;
constexpr int SMAX = K0 + 31 + DMAX;
constexpr int clampi(int v, int lo, int hi) { return v < lo ? lo : v > hi ? hi : v; }
constexpr int wo_lo(int n) { return clampi(((n - WO0) * WON + WOD - 1) / WOD, 0, 32); }
constexpr int extras(int n) { return wo_lo(n + 1) - wo_lo(n) + (n == DINIT - 1 ? 3 : 0) + (n == QPUB) + (n == QLD); }
constexpr int pace(int n, int d) { if (d > n) return 63; int c = d - 1; for (int i = n - d; i < n; ++i) c += extras(i); return c; }
constexpr int units8(int n) { return 8 * n + wo_lo(n); }
constexpr int dith(int n) { constexpr int t[4] = {0, 2, 1, 3}; return t[n & 3]; }
constexpr int thr(int n, int d) {
  const int a = OFFS + (units8(n) * RATE) / 8, b = OFFSB + (units8(n) * RATEB) / 8, e = (units8(n) * XR4) / 8;
  return ((d == 3 ? a + ((b - a) * dith(n)) / 3 : d == 4 ? b + (e * dith(n)) / 3 : b + e) + (d - 3) * GAP) / 10;
}
constexpr int wo_pairs(int n) { return wo_lo(n - DMAX) / 2; }
constexpr int QCMP = DINIT + DMAX, QGOT = QLD + DMAX + 1;
constexpr int pvdone(int n) {
  int d = 0;
  for (int i = SMAX + 1; i <= n; ++i) d = clampi(i - DMAX - V0 + 1, 0, d + 2);
  return d;
}
static_assert(QCMP < QPUB && QPUB < QLD && QGOT < QRD && QRD + 3 <= SMAX - 3 && wo_pairs(NSTEP - 2) == 16 && SMAX < FRD && FRD < NSTEP - 2 && WO0 >= DINIT && DINIT >= K0, "schedule");

constexpr int WS_EPOCH = 0;
constexpr int WS_L = 4096;
constexpr int WS_Y = 8192;
constexpr int WS_Q = WS_Y + 256 * 2048;
constexpr int WS_END = WS_Q + 64 * 1024;
constexpr unsigned SPIN_CAP = 1u << 18;
constexpr int OOB = 0x7fffff00;

struct Params {
  const unsigned short* k[2];
  const unsigned short* wq[2];
  const unsigned short* x[2];
  unsigned char* ws;
  unsigned short* out;
  const unsigned short* v[2];
  const unsigned short* wo[2];
};

__device__ __forceinline__ float dot2(unsigned a, unsigned b, float acc) {
  return __builtin_amdgcn_fdot2_f32_bf16(__builtin_bit_cast(bf16x2, a), __builtin_bit_cast(bf16x2, b), acc, false);
}
__device__ __forceinline__ unsigned cvtpk(float lo, float hi) {
  bf16x2 r; r.x = (__bf16)lo; r.y = (__bf16)hi; return __builtin_bit_cast(unsigned, r);
}
__device__ __forceinline__ float rbf(float x) { return (float)(__bf16)x; }
template <int CTRL> __device__ __forceinline__ float dpp(float x) {
  return __builtin_bit_cast(float, __builtin_amdgcn_mov_dpp(__builtin_bit_cast(int, x), CTRL, 0xf, 0xf, true));
}
template <int CTRL> __device__ __forceinline__ int dppi(int x) { return __builtin_amdgcn_mov_dpp(x, CTRL, 0xf, 0xf, true); }
constexpr int XOR1 = 0xB1, XOR2 = 0x4E, XOR7 = 0x141, XOR8 = 0x128, ROR4 = 0x124;
__device__ __forceinline__ float tr32(float a, float b) {
  auto t = __builtin_amdgcn_permlane32_swap(__float_as_uint(a), __float_as_uint(b), false, false);
  return __uint_as_float(t[0]) + __uint_as_float(t[1]);
}
__device__ __forceinline__ float tr16(float a, float b) {
  auto t = __builtin_amdgcn_permlane16_swap(__float_as_uint(a), __float_as_uint(b), false, false);
  return __uint_as_float(t[0]) + __uint_as_float(t[1]);
}
__device__ __forceinline__ float xrow16_max(float x) {
  auto s = __builtin_amdgcn_permlane16_swap(__float_as_uint(x), __float_as_uint(x), false, false);
  x = fmaxf(__uint_as_float(s[0]), __uint_as_float(s[1]));
  auto t = __builtin_amdgcn_permlane32_swap(__float_as_uint(x), __float_as_uint(x), false, false);
  return fmaxf(__uint_as_float(t[0]), __uint_as_float(t[1]));
}
__device__ __forceinline__ float lo16(unsigned u) { return __builtin_bit_cast(float, u << 16); }
__device__ __forceinline__ float hi16(unsigned u) { return __builtin_bit_cast(float, u & 0xffff0000u); }
__device__ __forceinline__ rsrc_t mkr(const void* p, int bytes) { return __builtin_amdgcn_make_buffer_rsrc((void*)p, 0, bytes, 0x00020000); }
template <int AUX> __device__ __forceinline__ v4u ldb(rsrc_t r, int voff, int soff) { return __builtin_amdgcn_raw_buffer_load_b128(r, voff, soff, AUX); }
template <int AUX> __device__ __forceinline__ v2u ldb2(rsrc_t r, int voff, int soff) { return __builtin_amdgcn_raw_buffer_load_b64(r, voff, soff, AUX); }
constexpr int NT = 2, SC1 = 16;
__device__ __forceinline__ unsigned gpack(float v, unsigned tb) { return ((__float_as_uint(v) + 0x80u) & 0xffffff00u) | tb; }
__device__ __forceinline__ float gval(unsigned g) { return __uint_as_float(g & 0xffffff00u); }
__device__ __forceinline__ unsigned binc(unsigned e) { return ((e & 0x7f7f7f7fu) + 0x01010101u) ^ (e & 0x80808080u); }
__device__ __forceinline__ unsigned now() { return (unsigned)__builtin_amdgcn_s_memrealtime(); }

#define SB() __builtin_amdgcn_sched_barrier(0)
#define MEMFENCE() asm volatile("" ::: "memory")
#define PIN(x) asm volatile("" : "+v"(x))
#define BAR() do { SB(); asm volatile("s_waitcnt lgkmcnt(0)" ::: "memory"); __builtin_amdgcn_s_barrier(); asm volatile("" ::: "memory"); SB(); } while (0)
template <int N> struct IC { static constexpr int v = N; };
template <int LO, int HI, class F> __device__ __forceinline__ void sfor(F&& f) { if constexpr (LO < HI) { f(IC<LO>{}); sfor<LO + 1, HI>(f); } }
template <int N> __device__ __forceinline__ unsigned pacestep(unsigned t0) {
  unsigned d;
  asm volatile("s_waitcnt vmcnt(%2)\n\ts_memrealtime vcc\n\ts_waitcnt lgkmcnt(0)\n\ts_sub_i32 %0, vcc_lo, %1\n\t"
               "s_cmpk_ge_i32 %0, %6\n\ts_cbranch_scc1 1f\n\ts_waitcnt vmcnt(%3)\n\t"
               "s_cmpk_ge_i32 %0, %7\n\ts_cbranch_scc1 1f\n\ts_waitcnt vmcnt(%4)\n\t"
               "s_cmpk_ge_i32 %0, %8\n\ts_cbranch_scc1 1f\n\ts_waitcnt vmcnt(%5)\n1:"
               : "=s"(d)
               : "s"(t0), "n"(pace(N, 5)), "n"(pace(N, 4)), "n"(pace(N, 3)), "n"(pace(N, 2)), "n"(thr(N, 5)), "n"(thr(N, 4)), "n"(thr(N, 3))
               : "scc", "vcc");
  SB();
  return d;
}
#define STAMP(k)
#define STAMPOUT(slot)

constexpr int OPS = 36;
constexpr int L_QP = 0;
constexpr int L_OP = 1024;
constexpr int L_ML = L_OP + 16 * OPS;
constexpr int L_Q = L_ML + 16;
constexpr int L_FLG = L_Q + 64;
constexpr int L_END = L_FLG + 24;
constexpr int POLLW = 7;

__global__ void __launch_bounds__(512, 2) __attribute__((aligned(4096))) k_fused(Params P) {
  __shared__ __attribute__((aligned(16))) float lds[L_END];
  volatile LAS unsigned* flg = (volatile LAS unsigned*)(lds + L_FLG);
  const u64x8 ka = *(const CAS u64x8*)__builtin_amdgcn_kernarg_segment_ptr();
  SB();
  STAMP(0);
  const int tid = threadIdx.x, lane = tid & 63, w = __builtin_amdgcn_readfirstlane(tid >> 6);
  const int r = lane >> 4, c = lane & 15;
  const int bid = blockIdx.x, xcd = bid & 7, jj = bid >> 3, hp = xcd * 8 + (jj >> 2);
  const int br = hp >> 5, h = hp & 31, b = (jj >> 1) & 1, sp = jj & 1;
  const int grp = br * 2 + b, pidx = h * 2 + sp;
  constexpr float L2E = 1.4426950408889634f;
  unsigned kvoff = (((b * NH + h) * SK + (SK - WIN) + sp * 1024 + w * 16) * DH) * 2;
  unsigned wqoff = (64 * w * HD + h * DH + 32 * (jj & 3)) * 2, xoff = 64 * w * 2;
  int lo = lane * 16;
  int qo = 4 * (lane >> 2) * (HD * 2) + (lane & 3) * 16;
  const int oo = 8 * r * (DM * 2) + c * 8;
  if (lane < 3) flg[lane * 8 + w] = 0u;
  asm volatile("" : "+s"(kvoff), "+s"(wqoff), "+s"(xoff));
  asm volatile("" : "+v"(lo), "+v"(qo));
  SB();
  const int kvbytes = 1024 * 256 - w * 4096;
  const rsrc_t wqr = mkr((const void*)((br ? ka[3] : ka[2]) + wqoff), 63 * HD * 2 + 64);
  rsrc_t kr, vr, wor;
#define KVOFF(n) (((n) >> 2) * 32768 + ((n) & 3) * 1024)
#define LDK(n) ldb<NT>(kr, lo, KVOFF(n))
#define LDV(n) ldb<NT>(vr, lo, KVOFF(n))
#define LDQ(n) ldb<NT>(wqr, qo, (n) * (HD * 2))
#define LDO(n) ldb2<NT>(wor, oo, (((n) >> 3) * 32 + ((n) & 7)) * (DM * 2))

  v4u A[4], K[32], V[32];
  v2u O[32];
  asm volatile("" :: "s"(wqr));
  SB();
  STAMP(1);
  const unsigned long long t0l = __builtin_amdgcn_s_memrealtime();
  SB();
  sfor<0, 4>([&](auto J) { A[J.v] = LDQ(J.v); SB(); });
  kr = mkr((const void*)((br ? ka[1] : ka[0]) + kvoff), kvbytes);
  sfor<K0, DINIT>([&](auto I) { K[I.v - K0] = LDK(I.v - K0); SB(); });
  const rsrc_t xr = mkr((const void*)((br ? ka[5] : ka[4]) + xoff), DM * 2 + 128);
  const rsrc_t wsr = mkr((const void*)ka[6], WS_END);
  const v2u x0 = ldb2<NT>(xr, 8 * (lane >> 2), 0), x1 = ldb2<NT>(xr, 8 * (lane >> 2), DM * 2);
  const v2u epv = ldb2<SC1 | NT>(wsr, 0, WS_EPOCH + 256 * br);
  BAR();
  asm volatile("" :: "s"(t0l));
  const unsigned t0 = (unsigned)t0l;
  {
    const u64x4 kb = *((const CAS u64x4*)__builtin_amdgcn_kernarg_segment_ptr() + 2);
    vr = mkr((const void*)((br ? kb[1] : kb[0]) + kvoff), kvbytes);
    wor = mkr((const void*)((br ? kb[3] : kb[2]) + (h * DH * DM + 64 * w) * 2), 127 * DM * 2 + 128);
  }

  v4u q;
  float x[16], s[2], p[2], acc[8], m, lam;
  v2u qg;
  v8u WB[4][2];
  unsigned tg0, tg1;
  LAS unsigned* wrp; const LAS v4u* rdp; int yso, ysb, glo, sidx; unsigned tgb;
  unsigned short* outp = (unsigned short*)ka[7] + b * 1024 + br * 512 + pidx * 8;
#pragma unroll
  for (int i = 0; i < 8; ++i) acc[i] = 0.f;

  auto QK = [&](auto I) {
    constexpr int n = I.v;
    float d = 0.f;
#pragma unroll
    for (int k = 0; k < 4; ++k) d = dot2(K[n][k], q[k], d);
    x[n & 15] = d;
    PIN(x[n & 15]);
  };
  auto TR = [&](int hf) {
#define TR_STEP(HALF, CTRL, BIT) _Pragma("unroll") for (int hh = 0; hh < (HALF); ++hh) { \
      const float lo_ = x[hh], hi_ = x[hh + (HALF)]; const float t1_ = lo_ + dpp<CTRL>(lo_), t2_ = hi_ + dpp<CTRL>(hi_); \
      x[hh] = (c & (BIT)) ? t2_ : t1_; }
    TR_STEP(8, XOR8, 8) TR_STEP(4, XOR7, 4) TR_STEP(2, XOR2, 2) TR_STEP(1, XOR1, 1)
#undef TR_STEP
    s[hf] = rbf(rbf(x[0]) * 0.08838834764831845f);
    PIN(s[hf]);
  };
  auto PV = [&](auto I) {
    constexpr int j = I.v;
    const float pj = dpp<0x150 + (j & 15)>(p[j >> 4]);
#pragma unroll
    for (int k = 0; k < 4; ++k) { acc[2 * k] = fmaf(pj, lo16(V[j][k]), acc[2 * k]); acc[2 * k + 1] = fmaf(pj, hi16(V[j][k]), acc[2 * k + 1]); }
#pragma unroll
    for (int k = 0; k < 8; ++k) PIN(acc[k]);
    SB();
  };
  auto WP = [&](auto I) {
    constexpr int i = I.v;
    WB[0][i >> 3][i & 7] = __builtin_amdgcn_perm(O[2 * i + 1][0], O[2 * i][0], 0x05040100u);
    WB[1][i >> 3][i & 7] = __builtin_amdgcn_perm(O[2 * i + 1][0], O[2 * i][0], 0x07060302u);
    WB[2][i >> 3][i & 7] = __builtin_amdgcn_perm(O[2 * i + 1][1], O[2 * i][1], 0x05040100u);
    WB[3][i >> 3][i & 7] = __builtin_amdgcn_perm(O[2 * i + 1][1], O[2 * i][1], 0x07060302u);
    if constexpr ((i & 7) == 7) { PIN(WB[0][i >> 3]); PIN(WB[1][i >> 3]); PIN(WB[2][i >> 3]); PIN(WB[3][i >> 3]); }
  };

  sfor<DINIT, NSTEP>([&](auto I) {
    constexpr int n = I.v;
    const unsigned tn = pacestep<n>(t0);
    if constexpr (n < V0) K[n - K0] = LDK(n - K0); else V[n - V0] = LDV(n - V0);
    if constexpr (n == QLD) qg = ldb2<QLA>(wsr, w == 1 ? lane * 8 : OOB, WS_Q + hp * 1024 + b * 512);
    SB();
    sfor<wo_lo(n), wo_lo(n + 1)>([&](auto J) { O[J.v] = LDO(J.v); });
    if constexpr (n == NSTEP - 1)
      __builtin_amdgcn_raw_buffer_store_b128(v4u{__float_as_uint(lam), tg0, tn + t0, tg1}, wsr, tid ? OOB : 0, WS_L + (grp * 64 + pidx) * 16, SC1);
    SB();
    if constexpr (n == QCMP) {
      float qa[16];
#pragma unroll
      for (int k = 0; k < 4; ++k) {
        const unsigned l0 = __builtin_amdgcn_perm(A[1][k], A[0][k], 0x05040100u), h0 = __builtin_amdgcn_perm(A[1][k], A[0][k], 0x07060302u);
        const unsigned l1 = __builtin_amdgcn_perm(A[3][k], A[2][k], 0x05040100u), h1 = __builtin_amdgcn_perm(A[3][k], A[2][k], 0x07060302u);
        qa[2 * k] = dot2(l1, x0[1], dot2(l0, x0[0], 0.f));
        qa[2 * k + 1] = dot2(h1, x0[1], dot2(h0, x0[0], 0.f));
        qa[8 + 2 * k] = dot2(l1, x1[1], dot2(l0, x1[0], 0.f));
        qa[8 + 2 * k + 1] = dot2(h1, x1[1], dot2(h0, x1[0], 0.f));
      }
      float t[8];
#pragma unroll
      for (int i = 0; i < 8; ++i) t[i] = tr32(qa[i], qa[8 + i]);
      f32x4 u;
#pragma unroll
      for (int i = 0; i < 4; ++i) { u[i] = tr16(t[i], t[4 + i]); u[i] += dpp<XOR8>(u[i]); u[i] += dpp<ROR4>(u[i]); }
      if (!(lane & 12)) *(f32x4*)(lds + L_QP + w * 64 + (r >> 1) * 32 + 8 * (lane & 3) + 4 * (r & 1)) = u;
      MEMFENCE();
      if (lane == 0) flg[w] = 1u;
      tg0 = binc(__builtin_amdgcn_readfirstlane(epv[0])); tg1 = binc(__builtin_amdgcn_readfirstlane(epv[1]));
      asm volatile("" : "+s"(outp), "+s"(tg0), "+s"(tg1));
    }
    if constexpr (n == QPUB) {
      f32x2 a = {0.f, 0.f};
      if (w == 0) {
        while (!__all(flg[lane & 7] != 0u)) __builtin_amdgcn_s_sleep(1);
        MEMFENCE();
#pragma unroll
        for (int ww = 0; ww < 8; ++ww) a += *(const f32x2*)(lds + L_QP + ww * 64 + 2 * (lane & 31));
      }
      __builtin_amdgcn_raw_buffer_store_b64(v2u{cvtpk(a[0], a[1]), (lane & 1) ? tg1 : tg0}, wsr, tid < 32 ? r * 512 + c * 8 : OOB, WS_Q + hp * 1024 + (jj & 3) * 128, QSA);
    }
    if constexpr (n == QGOT) {
      if (w == 1) {
        const unsigned want = (lane & 1) ? tg1 : tg0;
        if (__builtin_expect(__builtin_amdgcn_ballot_w64(qg[1] != want) != 0ull, 0)) {
          unsigned spins = 0;
#pragma nounroll
          do {
            __builtin_amdgcn_s_sleep(8); MEMFENCE();
            qg = ldb2<SC1>(wsr, lane * 8, WS_Q + hp * 1024 + b * 512);
          } while (__builtin_amdgcn_ballot_w64(qg[1] != want) != 0ull && ++spins < SPIN_CAP);
        }
        ((unsigned*)lds)[L_Q + lane] = qg[0];
        MEMFENCE();
        if (lane == 0) flg[16] = 1u;
      }
    }
    if constexpr (n == QRD) {
      STAMP(2);
      while (flg[16] == 0u) __builtin_amdgcn_s_sleep(1);
      MEMFENCE();
      q = *(const v4u*)(lds + L_Q + 4 * c);
      PIN(q);
    }
    if constexpr (n > QRD && n <= QRD + 2) sfor<8 * (n - QRD - 1), 8 * (n - QRD - 1) + 8>(QK);
    if constexpr (n == QRD + 3) TR(0);
    if constexpr (n >= SMAX - 3 && n <= SMAX) sfor<16 + 4 * (n - (SMAX - 3)), 20 + 4 * (n - (SMAX - 3))>(QK);
    if constexpr (n == SMAX) {
      TR(1);
      m = fmaxf(s[0], s[1]);
      m = fmaxf(m, dpp<XOR1>(m)); m = fmaxf(m, dpp<XOR2>(m)); m = fmaxf(m, dpp<XOR7>(m)); m = fmaxf(m, dpp<XOR8>(m));
      m = xrow16_max(m);
      p[0] = __builtin_amdgcn_exp2f((s[0] - m) * L2E);
      p[1] = __builtin_amdgcn_exp2f((s[1] - m) * L2E);
      float l = p[0] + p[1];
      l += dpp<XOR1>(l); l += dpp<XOR2>(l); l += dpp<XOR7>(l); l += dpp<XOR8>(l);
      l = tr16(l, l); l = tr32(l, l);
      if (lane == 0) { *(f32x2*)(lds + L_ML + 2 * w) = f32x2{m, l}; MEMFENCE(); flg[8 + w] = 1u; }
      STAMP(3);
    }
    if constexpr (n == FRD) {
      while (!__all(flg[8 + (lane & 7)] != 0u)) __builtin_amdgcn_s_sleep(1);
      MEMFENCE();
      const f32x2 ml = *(const f32x2*)(lds + L_ML + 2 * (lane & 7));
      float M = ml[0];
      M = fmaxf(M, dpp<XOR1>(M)); M = fmaxf(M, dpp<XOR2>(M)); M = fmaxf(M, dpp<XOR7>(M));
      float L = ml[1] * __builtin_amdgcn_exp2f((ml[0] - M) * L2E);
      L += dpp<XOR1>(L); L += dpp<XOR2>(L); L += dpp<XOR7>(L);
      const float f = __builtin_amdgcn_exp2f((m - M) * L2E) / L;
      p[0] *= f; p[1] *= f;
#pragma unroll
      for (int k = 0; k < 8; ++k) { acc[k] *= f; PIN(acc[k]); }
      lam = fmaf(M, L2E, __builtin_amdgcn_logf(L));
      PIN(lam); PIN(p[0]); PIN(p[1]);
    }
    sfor<wo_pairs(n - 1), wo_pairs(n)>(WP);
    if constexpr (n == NSTEP - 2) {
      wrp = (LAS unsigned*)lds + L_OP + (w + 8 * (r & 1)) * OPS + 2 * c + (r >> 1);
      rdp = (const LAS v4u*)((LAS unsigned*)lds + L_OP + c * OPS + 4 * r);
      sidx = (c & 8) ? 0xEEEE : 0x4444;
      yso = (4 * c + r) * 4;
      ysb = WS_Y + (grp * 64 + pidx) * 2048 + 256 * w;
      tgb = (((c & 1) ? tg1 : tg0) >> (8 * r)) & 0xffu;
      glo = w == POLLW ? lo : OOB;
      asm volatile("" : "+v"(wrp), "+v"(rdp), "+v"(yso), "+v"(tgb), "+v"(glo), "+v"(sidx));
      asm volatile("" : "+s"(ysb));
    }
    SB();
    if constexpr (n > SMAX) sfor<pvdone(n - 1), pvdone(n)>(PV);
    SB();
  });
  f32x4 d[4];
#pragma unroll
  for (int t = 0; t < 4; ++t) { d[t] = f32x4{0.f, 0.f, 0.f, 0.f}; PIN(d[t]); }
  sfor<pvdone(NSTEP - 1), 32>(PV);
  STAMP(4);
  {
    float t[4];
#pragma unroll
    for (int i = 0; i < 4; ++i) t[i] = tr32(acc[i], acc[4 + i]);
    *wrp = cvtpk(tr16(t[0], t[2]), tr16(t[1], t[3]));
  }
  SB();
  v4u gl = ldb<SC1>(wsr, glo, WS_L + grp * 1024);
  BAR();
  STAMP(5);
  {
    const v4u af0 = rdp[0], af1 = rdp[4];
    SB();
#pragma unroll
    for (int t = 0; t < 4; ++t) d[t] = __builtin_amdgcn_smfmac_f32_16x16x64_bf16(__builtin_bit_cast(bf16x8, af0), __builtin_bit_cast(bf16x16, WB[t][0]), d[t], sidx, 0, 0);
    SB();
#pragma unroll
    for (int t = 0; t < 4; ++t) d[t] = __builtin_amdgcn_smfmac_f32_16x16x64_bf16(__builtin_bit_cast(bf16x8, af1), __builtin_bit_cast(bf16x16, WB[t][1]), d[t], sidx, 0, 0);
    SB();
    float e[4];
#pragma unroll
    for (int t = 0; t < 4; ++t) { const f32x2 h2 = f32x2{d[t][0], d[t][1]} + f32x2{d[t][2], d[t][3]}; e[t] = h2[0] + h2[1]; }
    const float y = tr16(tr32(e[0], e[2]), tr32(e[1], e[3]));
    __builtin_amdgcn_raw_buffer_store_b32(gpack(y, tgb), wsr, yso, ysb, SC1);
  }
  STAMP(6);
  if (w == POLLW) {
    const unsigned ts = now();
    unsigned spins = 0;
    if (__builtin_amdgcn_ballot_w64(((gl[1] ^ tg0) | (gl[3] ^ tg1)) != 0u) != 0ull) {
#pragma nounroll
      do {
        MEMFENCE();
        gl = ldb<SC1>(wsr, lo, WS_L + grp * 1024);
      } while (__builtin_amdgcn_ballot_w64(((gl[1] ^ tg0) | (gl[3] ^ tg1)) != 0u) != 0ull && ++spins < SPIN_CAP);
    }
    const float la = __uint_as_float(gl[0]);
    const float wl = __builtin_amdgcn_rcpf(1.0f + __builtin_amdgcn_exp2f(dpp<XOR1>(la) - la));
    float wt[2];
#pragma unroll
    for (int i = 0; i < 2; ++i) wt[i] = __int_as_float(__builtin_amdgcn_ds_bpermute(((lane >> 1) + 32 * i) * 4, __float_as_int(wl)));
    int dl = (int)(gl[2] - ts), dm = dl;
    dl = max(dl, dppi<XOR1>(dl)); dl = max(dl, dppi<XOR2>(dl)); dl = max(dl, dppi<XOR7>(dl)); dl = max(dl, dppi<XOR8>(dl));
    dl = max(max(__builtin_amdgcn_readlane(dl, 0), __builtin_amdgcn_readlane(dl, 16)), max(__builtin_amdgcn_readlane(dl, 32), __builtin_amdgcn_readlane(dl, 48)));
    dm += dppi<XOR1>(dm); dm += dppi<XOR2>(dm); dm += dppi<XOR7>(dm); dm += dppi<XOR8>(dm);
    dm = ((__builtin_amdgcn_readlane(dm, 0) + __builtin_amdgcn_readlane(dm, 16)) + (__builtin_amdgcn_readlane(dm, 32) + __builtin_amdgcn_readlane(dm, 48))) >> 6;
    dl = min(max(max(dl + PLEAD, dm + PLMEAN), PMIN), 400);
    int yo = (lane >> 1) * 2048 + (lane & 1) * 16, yb = WS_Y + grp * 64 * 2048 + pidx * 32, yb1 = yb + 32 * 2048;
    unsigned want = (lane & 1) ? tg1 : tg0;
    asm volatile("" : "+s"(yb), "+s"(yb1));
    const rsrc_t outr = mkr(outp, 16);
    int oso = (lane & 14) ? OOB : (lane & 1) * 8 + (lane >> 4) * 2;
    asm volatile("" : "+v"(yo), "+v"(want), "+v"(oso), "+v"(wt[0]), "+v"(wt[1]));
    while ((int)(now() - ts) < dl) {}
    v4u gy[2];
#pragma nounroll
    for (;;) {
      MEMFENCE();
      gy[0] = ldb<SC1>(wsr, yo, yb); gy[1] = ldb<SC1>(wsr, yo, yb1);
      unsigned bad = 0;
#pragma unroll
      for (int i = 0; i < 2; ++i) bad |= (__builtin_amdgcn_perm(gy[i][1], gy[i][0], 0x0c0c0400u) | __builtin_amdgcn_perm(gy[i][3], gy[i][2], 0x04000c0cu)) ^ want;
      if (__builtin_amdgcn_ballot_w64(bad != 0u) == 0ull) break;
      if (++spins >= SPIN_CAP) break;
    }
    float a[4];
#pragma unroll
    for (int k = 0; k < 4; ++k) a[k] = fmaf(wt[1], gval(gy[1][k]), wt[0] * gval(gy[0][k]));
    float z = tr16(tr32(a[0], a[2]), tr32(a[1], a[3]));
    z += dpp<XOR2>(z); z += dpp<ROR4>(z); z += dpp<XOR8>(z);
    __builtin_amdgcn_raw_buffer_store_b16((unsigned short)cvtpk(z, z), outr, oso, 0, NT);
    if (lane == 0 && pidx == 0 && b == 0) __builtin_amdgcn_raw_buffer_store_b64(v2u{tg0, tg1}, wsr, 0, WS_EPOCH + 256 * br, SC1);
  }
  STAMP(7);
  STAMPOUT(bid * 8 + w);
}

extern "C" void kernel_launch(void* const* d_in, const int* in_sizes, int n_in,
                              void* d_out, int out_size, void* d_ws, size_t ws_size,
                              hipStream_t stream) {
  Params P;
  for (int i = 0; i < 2; ++i) {
    P.x[i] = (const unsigned short*)d_in[5 * i + 0];
    P.k[i] = (const unsigned short*)d_in[5 * i + 1];
    P.v[i] = (const unsigned short*)d_in[5 * i + 2];
    P.wq[i] = (const unsigned short*)d_in[5 * i + 3];
    P.wo[i] = (const unsigned short*)d_in[5 * i + 4];
  }
  P.out = (unsigned short*)d_out;
  P.ws = (unsigned char*)d_ws;
  hipLaunchKernelGGL(k_fused, dim3(256), dim3(512), 0, stream, P);
}
```

```cpp
#include <hip/hip_runtime.h>
#include <hip/hip_bf16.h>
#include <stdint.h>

typedef unsigned v4u __attribute__((ext_vector_type(4)));
typedef unsigned v2u __attribute__((ext_vector_type(2)));
typedef unsigned v8u __attribute__((ext_vector_type(8)));
typedef float f32x4 __attribute__((ext_vector_type(4)));
typedef float f32x2 __attribute__((ext_vector_type(2)));
typedef __bf16 bf16x2 __attribute__((ext_vector_type(2)));
typedef __bf16 bf16x8 __attribute__((ext_vector_type(8)));
typedef __bf16 bf16x16 __attribute__((ext_vector_type(16)));
typedef unsigned long long u64x8 __attribute__((ext_vector_type(8)));
typedef unsigned long long u64x4 __attribute__((ext_vector_type(4)));
typedef __amdgpu_buffer_rsrc_t rsrc_t;
#define LAS __attribute__((address_space(3)))
#define CAS __attribute__((address_space(4)))

constexpr int SK = 8192, WIN = 2048, DH = 128, NH = 32, DM = 512, HD = 4096;

#define DINIT 4
#define RATE 240
#define OFFS -150
#define RATEB 282
#define OFFSB 200
#define XR4 22
#define GAP 120
#define QRD 27
#define WO0 23
#define WON 1
#define WQD 1
#define WQN 1
#define FRD 50
#define PMIN 12
#define PLEAD 125
#define PLMEAN 187
constexpr int DMAX = 5;
constexpr int SMAX = (31 + DMAX > QRD + 7) ? 31 + DMAX : QRD + 7;
constexpr int clampi(int v, int lo, int hi) { return v < lo ? lo : v > hi ? hi : v; }
constexpr int wq_lo(int n) { return clampi((n * WQN + WQD - 1) / WQD, 0, 16); }
constexpr int wo_lo(int n) { return clampi((n - WO0) * WON, 0, 32); }
constexpr int extras(int n) {
  return n < DINIT - 1 ? 0 : n == DINIT - 1 ? wq_lo(DINIT) + 3 : wo_lo(n + 1) - wo_lo(n) + wq_lo(n + 1) - wq_lo(n);
}
constexpr int pace(int n, int d) { if (d > n) return 63; int c = d - 1; for (int i = n - d; i < n; ++i) c += extras(i); return c; }
constexpr int units8(int n) { return 8 * n + 2 * wq_lo(n) + wo_lo(n); }
constexpr int dith(int n) { constexpr int t[4] = {0, 2, 1, 3}; return t[n & 3]; }
constexpr int thr(int n, int d) {
  const int a = OFFS + (units8(n) * RATE) / 8, b = OFFSB + (units8(n) * RATEB) / 8, e = (units8(n) * XR4) / 8;
  return ((d == 3 ? a + ((b - a) * dith(n)) / 3 : d == 4 ? b + (e * dith(n)) / 3 : b + e) + (d - 3) * GAP) / 10;
}
constexpr int wq_pairs(int n) { return wq_lo(n - DMAX) / 2; }
constexpr int wo_pairs(int n) { return wo_lo(n - DMAX) / 2; }
constexpr int qpub() { int n = 0; while (wq_pairs(n) < 8) ++n; return n; }
constexpr int QPUB = qpub();
constexpr int pvdone(int n) {
  int d = 0;
  for (int i = SMAX + 1; i <= n; ++i) d = clampi(i - DMAX - 31, 0, d + 2);
  return d;
}
static_assert(QPUB < QRD && QRD + 3 <= SMAX - 3 && wo_pairs(63) == 16 && SMAX < FRD && FRD < 62 && WO0 >= DINIT, "schedule");

constexpr int WS_EPOCH = 0;
constexpr int WS_L = 4096;
constexpr int WS_Y = 8192;
constexpr int WS_END = WS_Y + 256 * 2048;
constexpr unsigned SPIN_CAP = 1u << 18;
constexpr int OOB = 0x7fffff00;

struct Params {
  const unsigned short* k[2];
  const unsigned short* wq[2];
  const unsigned short* x[2];
  unsigned char* ws;
  unsigned short* out;
  const unsigned short* v[2];
  const unsigned short* wo[2];
};

__device__ __forceinline__ float dot2(unsigned a, unsigned b, float acc) {
  return __builtin_amdgcn_fdot2_f32_bf16(__builtin_bit_cast(bf16x2, a), __builtin_bit_cast(bf16x2, b), acc, false);
}
__device__ __forceinline__ unsigned cvtpk(float lo, float hi) {
  bf16x2 r; r.x = (__bf16)lo; r.y = (__bf16)hi; return __builtin_bit_cast(unsigned, r);
}
__device__ __forceinline__ float rbf(float x) { return (float)(__bf16)x; }
template <int CTRL> __device__ __forceinline__ float dpp(float x) {
  return __builtin_bit_cast(float, __builtin_amdgcn_mov_dpp(__builtin_bit_cast(int, x), CTRL, 0xf, 0xf, true));
}
template <int CTRL> __device__ __forceinline__ int dppi(int x) { return __builtin_amdgcn_mov_dpp(x, CTRL, 0xf, 0xf, true); }
constexpr int XOR1 = 0xB1, XOR2 = 0x4E, XOR7 = 0x141, XOR8 = 0x128, ROR4 = 0x124;
__device__ __forceinline__ float tr32(float a, float b) {
  auto t = __builtin_amdgcn_permlane32_swap(__float_as_uint(a), __float_as_uint(b), false, false);
  return __uint_as_float(t[0]) + __uint_as_float(t[1]);
}
__device__ __forceinline__ float tr16(float a, float b) {
  auto t = __builtin_amdgcn_permlane16_swap(__float_as_uint(a), __float_as_uint(b), false, false);
  return __uint_as_float(t[0]) + __uint_as_float(t[1]);
}
__device__ __forceinline__ float xrow16_max(float x) {
  auto s = __builtin_amdgcn_permlane16_swap(__float_as_uint(x), __float_as_uint(x), false, false);
  x = fmaxf(__uint_as_float(s[0]), __uint_as_float(s[1]));
  auto t = __builtin_amdgcn_permlane32_swap(__float_as_uint(x), __float_as_uint(x), false, false);
  return fmaxf(__uint_as_float(t[0]), __uint_as_float(t[1]));
}
__device__ __forceinline__ float lo16(unsigned u) { return __builtin_bit_cast(float, u << 16); }
__device__ __forceinline__ float hi16(unsigned u) { return __builtin_bit_cast(float, u & 0xffff0000u); }
__device__ __forceinline__ rsrc_t mkr(const void* p, int bytes) { return __builtin_amdgcn_make_buffer_rsrc((void*)p, 0, bytes, 0x00020000); }
template <int AUX> __device__ __forceinline__ v4u ldb(rsrc_t r, int voff, int soff) { return __builtin_amdgcn_raw_buffer_load_b128(r, voff, soff, AUX); }
template <int AUX> __device__ __forceinline__ v2u ldb2(rsrc_t r, int voff, int soff) { return __builtin_amdgcn_raw_buffer_load_b64(r, voff, soff, AUX); }
constexpr int NT = 2, SC1 = 16;
__device__ __forceinline__ unsigned gpack(float v, unsigned tb) { return ((__float_as_uint(v) + 0x80u) & 0xffffff00u) | tb; }
__device__ __forceinline__ float gval(unsigned g) { return __uint_as_float(g & 0xffffff00u); }
__device__ __forceinline__ unsigned binc(unsigned e) { return ((e & 0x7f7f7f7fu) + 0x01010101u) ^ (e & 0x80808080u); }
__device__ __forceinline__ unsigned now() { return (unsigned)__builtin_amdgcn_s_memrealtime(); }

#define SB() __builtin_amdgcn_sched_barrier(0)
#define MEMFENCE() asm volatile("" ::: "memory")
#define PIN(x) asm volatile("" : "+v"(x))
#define BAR() do { SB(); asm volatile("s_waitcnt lgkmcnt(0)" ::: "memory"); __builtin_amdgcn_s_barrier(); asm volatile("" ::: "memory"); SB(); } while (0)
template <int N> struct IC { static constexpr int v = N; };
template <int LO, int HI, class F> __device__ __forceinline__ void sfor(F&& f) { if constexpr (LO < HI) { f(IC<LO>{}); sfor<LO + 1, HI>(f); } }
template <int N> __device__ __forceinline__ unsigned pacestep(unsigned t0) {
  unsigned d;
  asm volatile("s_waitcnt vmcnt(%2)\n\ts_memrealtime vcc\n\ts_waitcnt lgkmcnt(0)\n\ts_sub_i32 %0, vcc_lo, %1\n\t"
               "s_cmpk_ge_i32 %0, %6\n\ts_cbranch_scc1 1f\n\ts_waitcnt vmcnt(%3)\n\t"
               "s_cmpk_ge_i32 %0, %7\n\ts_cbranch_scc1 1f\n\ts_waitcnt vmcnt(%4)\n\t"
               "s_cmpk_ge_i32 %0, %8\n\ts_cbranch_scc1 1f\n\ts_waitcnt vmcnt(%5)\n1:"
               : "=s"(d)
               : "s"(t0), "n"(pace(N, 5)), "n"(pace(N, 4)), "n"(pace(N, 3)), "n"(pace(N, 2)), "n"(thr(N, 5)), "n"(thr(N, 4)), "n"(thr(N, 3))
               : "scc", "vcc");
  SB();
  return d;
}
#define STAMP(k)
#define STAMPOUT(slot)

constexpr int OPS = 36;
constexpr int L_QP = 0;
constexpr int L_OP = 1024;
constexpr int L_ML = L_OP + 16 * OPS;
constexpr int L_FLG = L_ML + 16;
constexpr int L_END = L_FLG + 16;
constexpr int POLLW = 7;

__global__ void __launch_bounds__(512, 2) __attribute__((aligned(4096))) k_fused(Params P) {
  __shared__ __attribute__((aligned(16))) float lds[L_END];
  volatile LAS unsigned* flg = (volatile LAS unsigned*)(lds + L_FLG);
  const u64x8 ka = *(const CAS u64x8*)__builtin_amdgcn_kernarg_segment_ptr();
  SB();
  STAMP(0);
  const int tid = threadIdx.x, lane = tid & 63, w = __builtin_amdgcn_readfirstlane(tid >> 6);
  const int r = lane >> 4, c = lane & 15;
  const int bid = blockIdx.x, xcd = bid & 7, jj = bid >> 3, hp = xcd * 8 + (jj >> 2);
  const int br = hp >> 5, h = hp & 31, b = (jj >> 1) & 1, sp = jj & 1;
  const int grp = br * 2 + b, pidx = h * 2 + sp;
  constexpr float L2E = 1.4426950408889634f;
  unsigned kvoff = (((b * NH + h) * SK + (SK - WIN) + sp * 1024 + w * 16) * DH) * 2;
  unsigned wqoff = (64 * w * HD + h * DH) * 2, xoff = (b * DM + 64 * w) * 2;
  int lo = lane * 16;
  int qo = 16 * r * (HD * 2) + c * 16;
  const int oo = 8 * r * (DM * 2) + c * 8;
  if (lane < 2) flg[lane * 8 + w] = 0u;
  asm volatile("" : "+s"(kvoff), "+s"(wqoff), "+s"(xoff));
  asm volatile("" : "+v"(lo), "+v"(qo));
  SB();
  const int kvbytes = 1024 * 256 - w * 4096;
  const rsrc_t kr = mkr((const void*)((br ? ka[1] : ka[0]) + kvoff), kvbytes);
  rsrc_t wqr, vr, wor;
#define KVOFF(n) (((n) >> 2) * 32768 + ((n) & 3) * 1024)
#define LDK(n) ldb<NT>(kr, lo, KVOFF(n))
#define LDV(n) ldb<NT>(vr, lo, KVOFF(n))
#define LDQ(n) ldb<NT>(wqr, qo, (n) * (HD * 2))
#define LDO(n) ldb2<NT>(wor, oo, (((n) >> 3) * 32 + ((n) & 7)) * (DM * 2))

  v4u A[16], K[32], V[32];
  v2u O[32];
  asm volatile("" :: "s"(kr));
  SB();
  STAMP(1);
  const unsigned long long t0l = __builtin_amdgcn_s_memrealtime();
  SB();
  sfor<0, DINIT>([&](auto I) { K[I.v] = LDK(I.v); SB(); });
  wqr = mkr((const void*)((br ? ka[3] : ka[2]) + wqoff), 63 * HD * 2 + DH * 2);
  sfor<0, wq_lo(DINIT)>([&](auto J) { A[J.v] = LDQ(J.v); SB(); });
  const rsrc_t xr = mkr((const void*)((br ? ka[5] : ka[4]) + xoff), 128);
  const rsrc_t wsr = mkr((const void*)ka[6], WS_END);
  const v4u xa = ldb<NT>(xr, 32 * r, 0), xb = ldb<NT>(xr, 32 * r, 16);
  const v2u epv = ldb2<SC1 | NT>(wsr, 0, WS_EPOCH + 256 * grp);
  BAR();
  asm volatile("" :: "s"(t0l));
  const unsigned t0 = (unsigned)t0l;
  {
    const u64x4 kb = *((const CAS u64x4*)__builtin_amdgcn_kernarg_segment_ptr() + 2);
    vr = mkr((const void*)((br ? kb[1] : kb[0]) + kvoff), kvbytes);
    wor = mkr((const void*)((br ? kb[3] : kb[2]) + (h * DH * DM + 64 * w) * 2), 127 * DM * 2 + 128);
  }

  v4u q;
  float qa[8], x[16], s[2], p[2], acc[8], m, lam;
  v8u WB[4][2];
  unsigned tg0, tg1;
  LAS unsigned* wrp; const LAS v4u* rdp; int yso, ysb, glo, sidx; unsigned tgb;
  unsigned short* outp = (unsigned short*)ka[7] + b * 1024 + br * 512 + pidx * 8;
#pragma unroll
  for (int i = 0; i < 8; ++i) acc[i] = qa[i] = 0.f;

  auto QP = [&](auto I) {
    constexpr int i = I.v;
    const unsigned xx = i < 4 ? xa[i & 3] : xb[i & 3];
#pragma unroll
    for (int k = 0; k < 4; ++k) {
      qa[2 * k] = dot2(__builtin_amdgcn_perm(A[2 * i + 1][k], A[2 * i][k], 0x05040100u), xx, qa[2 * k]);
      qa[2 * k + 1] = dot2(__builtin_amdgcn_perm(A[2 * i + 1][k], A[2 * i][k], 0x07060302u), xx, qa[2 * k + 1]);
    }
#pragma unroll
    for (int k = 0; k < 8; ++k) PIN(qa[k]);
  };
  auto QK = [&](auto I) {
    constexpr int n = I.v;
    float d = 0.f;
#pragma unroll
    for (int k = 0; k < 4; ++k) d = dot2(K[n][k], q[k], d);
    x[n & 15] = d;
    PIN(x[n & 15]);
  };
  auto TR = [&](int hf) {
#define TR_STEP(HALF, CTRL, BIT) _Pragma("unroll") for (int hh = 0; hh < (HALF); ++hh) { \
      const float lo_ = x[hh], hi_ = x[hh + (HALF)]; const float t1_ = lo_ + dpp<CTRL>(lo_), t2_ = hi_ + dpp<CTRL>(hi_); \
      x[hh] = (c & (BIT)) ? t2_ : t1_; }
    TR_STEP(8, XOR8, 8) TR_STEP(4, XOR7, 4) TR_STEP(2, XOR2, 2) TR_STEP(1, XOR1, 1)
#undef TR_STEP
    s[hf] = rbf(rbf(x[0]) * 0.08838834764831845f);
    PIN(s[hf]);
  };
  auto PV = [&](auto I) {
    constexpr int j = I.v;
    const float pj = dpp<0x150 + (j & 15)>(p[j >> 4]);
#pragma unroll
    for (int k = 0; k < 4; ++k) { acc[2 * k] = fmaf(pj, lo16(V[j][k]), acc[2 * k]); acc[2 * k + 1] = fmaf(pj, hi16(V[j][k]), acc[2 * k + 1]); }
#pragma unroll
    for (int k = 0; k < 8; ++k) PIN(acc[k]);
    SB();
  };
  auto WP = [&](auto I) {
    constexpr int i = I.v;
    WB[0][i >> 3][i & 7] = __builtin_amdgcn_perm(O[2 * i + 1][0], O[2 * i][0], 0x05040100u);
    WB[1][i >> 3][i & 7] = __builtin_amdgcn_perm(O[2 * i + 1][0], O[2 * i][0], 0x07060302u);
    WB[2][i >> 3][i & 7] = __builtin_amdgcn_perm(O[2 * i + 1][1], O[2 * i][1], 0x05040100u);
    WB[3][i >> 3][i & 7] = __builtin_amdgcn_perm(O[2 * i + 1][1], O[2 * i][1], 0x07060302u);
    if constexpr ((i & 7) == 7) { PIN(WB[0][i >> 3]); PIN(WB[1][i >> 3]); PIN(WB[2][i >> 3]); PIN(WB[3][i >> 3]); }
  };

  sfor<DINIT, 64>([&](auto I) {
    constexpr int n = I.v;
    const unsigned tn = pacestep<n>(t0);
    if constexpr (n < 32) K[n] = LDK(n); else V[n - 32] = LDV(n - 32);
    SB();
    sfor<wq_lo(n), wq_lo(n + 1)>([&](auto J) { A[J.v] = LDQ(J.v); });
    sfor<wo_lo(n), wo_lo(n + 1)>([&](auto J) { O[J.v] = LDO(J.v); });
    if constexpr (n == 63)
      __builtin_amdgcn_raw_buffer_store_b128(v4u{__float_as_uint(lam), tg0, tn + t0, tg1}, wsr, tid ? OOB : 0, WS_L + (grp * 64 + pidx) * 16, SC1);
    SB();
    if constexpr (n <= QPUB) sfor<wq_pairs(n - 1), wq_pairs(n)>(QP);
    if constexpr (n == QPUB) {
      float t[4];
#pragma unroll
      for (int i = 0; i < 4; ++i) t[i] = tr32(qa[i], qa[4 + i]);
      f32x2 u;
#pragma unroll
      for (int i = 0; i < 2; ++i) u[i] = tr16(t[i], t[2 + i]);
      *(f32x2*)(lds + L_QP + w * 128 + 8 * c + 2 * r) = u;
      MEMFENCE();
      if (lane == 0) flg[w] = 1u;
      tg0 = binc(__builtin_amdgcn_readfirstlane(epv[0])); tg1 = binc(__builtin_amdgcn_readfirstlane(epv[1]));
      asm volatile("" : "+s"(outp), "+s"(tg0), "+s"(tg1));
    }
    if constexpr (n == QRD) {
      STAMP(2);
      while (!__all(flg[lane & 7] != 0u)) __builtin_amdgcn_s_sleep(1);
      MEMFENCE();
      f32x4 q0 = {0.f, 0.f, 0.f, 0.f}, q1 = {0.f, 0.f, 0.f, 0.f};
#pragma unroll
      for (int ww = 0; ww < 8; ++ww) {
        q0 += *(const f32x4*)(lds + L_QP + ww * 128 + 8 * c);
        q1 += *(const f32x4*)(lds + L_QP + ww * 128 + 8 * c + 4);
      }
      q = v4u{cvtpk(q0[0], q0[1]), cvtpk(q0[2], q0[3]), cvtpk(q1[0], q1[1]), cvtpk(q1[2], q1[3])};
      PIN(q);
    }
    if constexpr (n > QRD && n <= QRD + 2) sfor<8 * (n - QRD - 1), 8 * (n - QRD - 1) + 8>(QK);
    if constexpr (n == QRD + 3) TR(0);
    if constexpr (n >= SMAX - 3 && n <= SMAX) sfor<16 + 4 * (n - (SMAX - 3)), 20 + 4 * (n - (SMAX - 3))>(QK);
    if constexpr (n == SMAX) {
      TR(1);
      m = fmaxf(s[0], s[1]);
      m = fmaxf(m, dpp<XOR1>(m)); m = fmaxf(m, dpp<XOR2>(m)); m = fmaxf(m, dpp<XOR7>(m)); m = fmaxf(m, dpp<XOR8>(m));
      m = xrow16_max(m);
      p[0] = __builtin_amdgcn_exp2f((s[0] - m) * L2E);
      p[1] = __builtin_amdgcn_exp2f((s[1] - m) * L2E);
      float l = p[0] + p[1];
      l += dpp<XOR1>(l); l += dpp<XOR2>(l); l += dpp<XOR7>(l); l += dpp<XOR8>(l);
      l = tr16(l, l); l = tr32(l, l);
      if (lane == 0) { *(f32x2*)(lds + L_ML + 2 * w) = f32x2{m, l}; MEMFENCE(); flg[8 + w] = 1u; }
      STAMP(3);
    }
    if constexpr (n == FRD) {
      while (!__all(flg[8 + (lane & 7)] != 0u)) __builtin_amdgcn_s_sleep(1);
      MEMFENCE();
      const f32x2 ml = *(const f32x2*)(lds + L_ML + 2 * (lane & 7));
      float M = ml[0];
      M = fmaxf(M, dpp<XOR1>(M)); M = fmaxf(M, dpp<XOR2>(M)); M = fmaxf(M, dpp<XOR7>(M));
      float L = ml[1] * __builtin_amdgcn_exp2f((ml[0] - M) * L2E);
      L += dpp<XOR1>(L); L += dpp<XOR2>(L); L += dpp<XOR7>(L);
      const float f = __builtin_amdgcn_exp2f((m - M) * L2E) / L;
      p[0] *= f; p[1] *= f;
#pragma unroll
      for (int k = 0; k < 8; ++k) { acc[k] *= f; PIN(acc[k]); }
      lam = fmaf(M, L2E, __builtin_amdgcn_logf(L));
      PIN(lam); PIN(p[0]); PIN(p[1]);
    }
    sfor<wo_pairs(n - 1), wo_pairs(n)>(WP);
    if constexpr (n == 62) {
      wrp = (LAS unsigned*)lds + L_OP + (w + 8 * (r & 1)) * OPS + 2 * c + (r >> 1);
      rdp = (const LAS v4u*)((LAS unsigned*)lds + L_OP + c * OPS + 4 * r);
      sidx = (c & 8) ? 0xEEEE : 0x4444;
      yso = (4 * c + r) * 4;
      ysb = WS_Y + (grp * 64 + pidx) * 2048 + 256 * w;
      tgb = (((c & 1) ? tg1 : tg0) >> (8 * r)) & 0xffu;
      glo = w == POLLW ? lo : OOB;
      asm volatile("" : "+v"(wrp), "+v"(rdp), "+v"(yso), "+v"(tgb), "+v"(glo), "+v"(sidx));
      asm volatile("" : "+s"(ysb));
    }
    SB();
    if constexpr (n > SMAX) sfor<pvdone(n - 1), pvdone(n)>(PV);
    SB();
  });
  f32x4 d[4];
#pragma unroll
  for (int t = 0; t < 4; ++t) { d[t] = f32x4{0.f, 0.f, 0.f, 0.f}; PIN(d[t]); }
  sfor<pvdone(63), 32>(PV);
  STAMP(4);
  {
    float t[4];
#pragma unroll
    for (int i = 0; i < 4; ++i) t[i] = tr32(acc[i], acc[4 + i]);
    *wrp = cvtpk(tr16(t[0], t[2]), tr16(t[1], t[3]));
  }
  SB();
  v4u gl = ldb<SC1>(wsr, glo, WS_L + grp * 1024);
  BAR();
  STAMP(5);
  {
    const v4u af0 = rdp[0], af1 = rdp[4];
    SB();
#pragma unroll
    for (int t = 0; t < 4; ++t) d[t] = __builtin_amdgcn_smfmac_f32_16x16x64_bf16(__builtin_bit_cast(bf16x8, af0), __builtin_bit_cast(bf16x16, WB[t][0]), d[t], sidx, 0, 0);
    SB();
#pragma unroll
    for (int t = 0; t < 4; ++t) d[t] = __builtin_amdgcn_smfmac_f32_16x16x64_bf16(__builtin_bit_cast(bf16x8, af1), __builtin_bit_cast(bf16x16, WB[t][1]), d[t], sidx, 0, 0);
    SB();
    float e[4];
#pragma unroll
    for (int t = 0; t < 4; ++t) { const f32x2 h2 = f32x2{d[t][0], d[t][1]} + f32x2{d[t][2], d[t][3]}; e[t] = h2[0] + h2[1]; }
    const float y = tr16(tr32(e[0], e[2]), tr32(e[1], e[3]));
    __builtin_amdgcn_raw_buffer_store_b32(gpack(y, tgb), wsr, yso, ysb, SC1);
  }
  STAMP(6);
  if (w == POLLW) {
    const unsigned ts = now();
    unsigned spins = 0;
    if (__builtin_amdgcn_ballot_w64(((gl[1] ^ tg0) | (gl[3] ^ tg1)) != 0u) != 0ull) {
#pragma nounroll
      do {
        MEMFENCE();
        gl = ldb<SC1>(wsr, lo, WS_L + grp * 1024);
      } while (__builtin_amdgcn_ballot_w64(((gl[1] ^ tg0) | (gl[3] ^ tg1)) != 0u) != 0ull && ++spins < SPIN_CAP);
    }
    const float la = __uint_as_float(gl[0]);
    const float wl = __builtin_amdgcn_rcpf(1.0f + __builtin_amdgcn_exp2f(dpp<XOR1>(la) - la));
    float wt[2];
#pragma unroll
    for (int i = 0; i < 2; ++i) wt[i] = __int_as_float(__builtin_amdgcn_ds_bpermute(((lane >> 1) + 32 * i) * 4, __float_as_int(wl)));
    int dl = (int)(gl[2] - ts), dm = dl;
    dl = max(dl, dppi<XOR1>(dl)); dl = max(dl, dppi<XOR2>(dl)); dl = max(dl, dppi<XOR7>(dl)); dl = max(dl, dppi<XOR8>(dl));
    dl = max(max(__builtin_amdgcn_readlane(dl, 0), __builtin_amdgcn_readlane(dl, 16)), max(__builtin_amdgcn_readlane(dl, 32), __builtin_amdgcn_readlane(dl, 48)));
    dm += dppi<XOR1>(dm); dm += dppi<XOR2>(dm); dm += dppi<XOR7>(dm); dm += dppi<XOR8>(dm);
    dm = ((__builtin_amdgcn_readlane(dm, 0) + __builtin_amdgcn_readlane(dm, 16)) + (__builtin_amdgcn_readlane(dm, 32) + __builtin_amdgcn_readlane(dm, 48))) >> 6;
    dl = min(max(max(dl + PLEAD, dm + PLMEAN), PMIN), 400);
    int yo = (lane >> 1) * 2048 + (lane & 1) * 16, yb = WS_Y + grp * 64 * 2048 + pidx * 32, yb1 = yb + 32 * 2048;
    unsigned want = (lane & 1) ? tg1 : tg0;
    asm volatile("" : "+s"(yb), "+s"(yb1));
    const rsrc_t outr = mkr(outp, 16);
    int oso = (lane & 14) ? OOB : (lane & 1) * 8 + (lane >> 4) * 2;
    asm volatile("" : "+v"(yo), "+v"(want), "+v"(oso), "+v"(wt[0]), "+v"(wt[1]));
    while ((int)(now() - ts) < dl) {}
    v4u gy[2];
#pragma nounroll
    for (;;) {
      MEMFENCE();
      gy[0] = ldb<SC1>(wsr, yo, yb); gy[1] = ldb<SC1>(wsr, yo, yb1);
      unsigned bad = 0;
#pragma unroll
      for (int i = 0; i < 2; ++i) bad |= (__builtin_amdgcn_perm(gy[i][1], gy[i][0], 0x0c0c0400u) | __builtin_amdgcn_perm(gy[i][3], gy[i][2], 0x04000c0cu)) ^ want;
      if (__builtin_amdgcn_ballot_w64(bad != 0u) == 0ull) break;
      if (++spins >= SPIN_CAP) break;
    }
    float a[4];
#pragma unroll
    for (int k = 0; k < 4; ++k) a[k] = fmaf(wt[1], gval(gy[1][k]), wt[0] * gval(gy[0][k]));
    float z = tr16(tr32(a[0], a[2]), tr32(a[1], a[3]));
    z += dpp<XOR2>(z); z += dpp<ROR4>(z); z += dpp<XOR8>(z);
    __builtin_amdgcn_raw_buffer_store_b16((unsigned short)cvtpk(z, z), outr, oso, 0, NT);
    if (lane == 0 && pidx == 0) __builtin_amdgcn_raw_buffer_store_b64(v2u{tg0, tg1}, wsr, 0, WS_EPOCH + 256 * grp, SC1);
  }
  STAMP(7);
  STAMPOUT(bid * 8 + w);
}

extern "C" void kernel_launch(void* const* d_in, const int* in_sizes, int n_in,
                              void* d_out, int out_size, void* d_ws, size_t ws_size,
                              hipStream_t stream) {
  Params P;
  for (int i = 0; i < 2; ++i) {
    P.x[i] = (const unsigned short*)d_in[5 * i + 0];
    P.k[i] = (const unsigned short*)d_in[5 * i + 1];
    P.v[i] = (const unsigned short*)d_in[5 * i + 2];
    P.wq[i] = (const unsigned short*)d_in[5 * i + 3];
    P.wo[i] = (const unsigned short*)d_in[5 * i + 4];
  }
  P.out = (unsigned short*)d_out;
  P.ws = (unsigned char*)d_ws;
  hipLaunchKernelGGL(k_fused, dim3(256), dim3(512), 0, stream, P);
}
```

```cpp
#include <hip/hip_runtime.h>
#include <hip/hip_bf16.h>
#include <stdint.h>

typedef unsigned v4u __attribute__((ext_vector_type(4)));
typedef unsigned v2u __attribute__((ext_vector_type(2)));
typedef unsigned v8u __attribute__((ext_vector_type(8)));
typedef float f32x4 __attribute__((ext_vector_type(4)));
typedef float f32x2 __attribute__((ext_vector_type(2)));
typedef __bf16 bf16x2 __attribute__((ext_vector_type(2)));
typedef __bf16 bf16x8 __attribute__((ext_vector_type(8)));
typedef __bf16 bf16x16 __attribute__((ext_vector_type(16)));
typedef unsigned long long u64x8 __attribute__((ext_vector_type(8)));
typedef unsigned long long u64x4 __attribute__((ext_vector_type(4)));
typedef __amdgpu_buffer_rsrc_t rsrc_t;
#define LAS __attribute__((address_space(3)))
#define CAS __attribute__((address_space(4)))

constexpr int SK = 8192, WIN = 2048, DH = 128, NH = 32, DM = 512, HD = 4096;

constexpr int DINIT = 5;
constexpr int RATE = 240, OFFS = -300;
constexpr int RATEB = 282, OFFSB = 50;
constexpr int XR4 = 22, GAP = 120;
constexpr int QPUB = 11;
constexpr int QLD = 24;
constexpr int QRD = 32;
constexpr int WO0 = 9, WON = 3, WOD = 2;
constexpr int FRD = 54;
constexpr int PMIN = 12;
constexpr int PLEAD = 125;
constexpr int PLMEAN = 187;
constexpr int DMAX = 5;
constexpr int K0 = 4, V0 = 36, NSTEP = 68;
constexpr int SMAX = K0 + 31 + DMAX;
constexpr int clampi(int v, int lo, int hi) { return v < lo ? lo : v > hi ? hi : v; }
constexpr int wo_lo(int n) { return clampi(((n - WO0) * WON + WOD - 1) / WOD, 0, 32); }
constexpr int extras(int n) { return wo_lo(n + 1) - wo_lo(n) + (n == DINIT - 1 ? 3 : 0) + (n == QPUB) + (n == QLD); }
constexpr int pace(int n, int d) { if (d > n) return 63; int c = d - 1; for (int i = n - d; i < n; ++i) c += extras(i); return c; }
constexpr int units8(int n) { return 8 * n + wo_lo(n); }
constexpr int dith(int n) { constexpr int t[4] = {0, 2, 1, 3}; return t[n & 3]; }
constexpr int thr(int n, int d) {
  const int a = OFFS + (units8(n) * RATE) / 8, b = OFFSB + (units8(n) * RATEB) / 8, e = (units8(n) * XR4) / 8;
  return ((d == 3 ? a + ((b - a) * dith(n)) / 3 : d == 4 ? b + (e * dith(n)) / 3 : b + e) + (d - 3) * GAP) / 10;
}
constexpr int wo_pairs(int n) { return wo_lo(n - DMAX) / 2; }
constexpr int QCMP = DINIT + DMAX, QGOT = QLD + DMAX + 1;
constexpr int pvdone(int n) {
  int d = 0;
  for (int i = SMAX + 1; i <= n; ++i) d = clampi(i - DMAX - V0 + 1, 0, d + 2);
  return d;
}
static_assert(QCMP < QPUB && QPUB < QLD && QGOT < QRD && QRD + 3 <= SMAX - 3 && wo_pairs(NSTEP - 2) == 16 && SMAX < FRD && FRD < NSTEP - 2 && WO0 >= DINIT && DINIT >= K0, "schedule");

constexpr int WS_EPOCH = 0;
constexpr int WS_L = 4096;
constexpr int WS_Y = 8192;
constexpr int WS_Q = WS_Y + 256 * 2048;
constexpr int WS_END = WS_Q + 64 * 1024;
constexpr unsigned SPIN_CAP = 1u << 18;
constexpr int OOB = 0x7fffff00;

struct Params {
  const unsigned short* k[2];
  const unsigned short* wq[2];
  const unsigned short* x[2];
  unsigned char* ws;
  unsigned short* out;
  const unsigned short* v[2];
  const unsigned short* wo[2];
};

__device__ __forceinline__ float dot2(unsigned a, unsigned b, float acc) {
  return __builtin_amdgcn_fdot2_f32_bf16(__builtin_bit_cast(bf16x2, a), __builtin_bit_cast(bf16x2, b), acc, false);
}
__device__ __forceinline__ unsigned cvtpk(float lo, float hi) {
  bf16x2 r; r.x = (__bf16)lo; r.y = (__bf16)hi; return __builtin_bit_cast(unsigned, r);
}
__device__ __forceinline__ float rbf(float x) { return (float)(__bf16)x; }
template <int CTRL> __device__ __forceinline__ float dpp(float x) {
  return __builtin_bit_cast(float, __builtin_amdgcn_mov_dpp(__builtin_bit_cast(int, x), CTRL, 0xf, 0xf, true));
}
template <int CTRL> __device__ __forceinline__ int dppi(int x) { return __builtin_amdgcn_mov_dpp(x, CTRL, 0xf, 0xf, true); }
constexpr int XOR1 = 0xB1, XOR2 = 0x4E, XOR7 = 0x141, XOR8 = 0x128, ROR4 = 0x124;
__device__ __forceinline__ float tr32(float a, float b) {
  auto t = __builtin_amdgcn_permlane32_swap(__float_as_uint(a), __float_as_uint(b), false, false);
  return __uint_as_float(t[0]) + __uint_as_float(t[1]);
}
__device__ __forceinline__ float tr16(float a, float b) {
  auto t = __builtin_amdgcn_permlane16_swap(__float_as_uint(a), __float_as_uint(b), false, false);
  return __uint_as_float(t[0]) + __uint_as_float(t[1]);
}
__device__ __forceinline__ float xrow16_max(float x) {
  auto s = __builtin_amdgcn_permlane16_swap(__float_as_uint(x), __float_as_uint(x), false, false);
  x = fmaxf(__uint_as_float(s[0]), __uint_as_float(s[1]));
  auto t = __builtin_amdgcn_permlane32_swap(__float_as_uint(x), __float_as_uint(x), false, false);
  return fmaxf(__uint_as_float(t[0]), __uint_as_float(t[1]));
}
__device__ __forceinline__ float lo16(unsigned u) { return __builtin_bit_cast(float, u << 16); }
__device__ __forceinline__ float hi16(unsigned u) { return __builtin_bit_cast(float, u & 0xffff0000u); }
__device__ __forceinline__ rsrc_t mkr(const void* p, int bytes) { return __builtin_amdgcn_make_buffer_rsrc((void*)p, 0, bytes, 0x00020000); }
template <int AUX> __device__ __forceinline__ v4u ldb(rsrc_t r, int voff, int soff) { return __builtin_amdgcn_raw_buffer_load_b128(r, voff, soff, AUX); }
template <int AUX> __device__ __forceinline__ v2u ldb2(rsrc_t r, int voff, int soff) { return __builtin_amdgcn_raw_buffer_load_b64(r, voff, soff, AUX); }
constexpr int NT = 2, SC1 = 16;
__device__ __forceinline__ unsigned gpack(float v, unsigned tb) { return ((__float_as_uint(v) + 0x80u) & 0xffffff00u) | tb; }
__device__ __forceinline__ float gval(unsigned g) { return __uint_as_float(g & 0xffffff00u); }
__device__ __forceinline__ unsigned binc(unsigned e) { return ((e & 0x7f7f7f7fu) + 0x01010101u) ^ (e & 0x80808080u); }
__device__ __forceinline__ unsigned now() { return (unsigned)__builtin_amdgcn_s_memrealtime(); }

#define SB() __builtin_amdgcn_sched_barrier(0)
#define MEMFENCE() asm volatile("" ::: "memory")
#define PIN(x) asm volatile("" : "+v"(x))
#define BAR() do { SB(); asm volatile("s_waitcnt lgkmcnt(0)" ::: "memory"); __builtin_amdgcn_s_barrier(); asm volatile("" ::: "memory"); SB(); } while (0)
template <int N> struct IC { static constexpr int v = N; };
template <int LO, int HI, class F> __device__ __forceinline__ void sfor(F&& f) { if constexpr (LO < HI) { f(IC<LO>{}); sfor<LO + 1, HI>(f); } }
template <int N> __device__ __forceinline__ unsigned pacestep(unsigned t0) {
  unsigned d;
  asm volatile("s_waitcnt vmcnt(%2)\n\ts_memrealtime vcc\n\ts_waitcnt lgkmcnt(0)\n\ts_sub_i32 %0, vcc_lo, %1\n\t"
               "s_cmpk_ge_i32 %0, %6\n\ts_cbranch_scc1 1f\n\ts_waitcnt vmcnt(%3)\n\t"
               "s_cmpk_ge_i32 %0, %7\n\ts_cbranch_scc1 1f\n\ts_waitcnt vmcnt(%4)\n\t"
               "s_cmpk_ge_i32 %0, %8\n\ts_cbranch_scc1 1f\n\ts_waitcnt vmcnt(%5)\n1:"
               : "=s"(d)
               : "s"(t0), "n"(pace(N, 5)), "n"(pace(N, 4)), "n"(pace(N, 3)), "n"(pace(N, 2)), "n"(thr(N, 5)), "n"(thr(N, 4)), "n"(thr(N, 3))
               : "scc", "vcc");
  SB();
  return d;
}

constexpr int OPS = 36;
constexpr int L_QP = 0;
constexpr int L_OP = 512;
constexpr int L_ML = L_OP + 16 * OPS;
constexpr int L_Q = L_ML + 16;
constexpr int L_FLG = L_Q + 64;
constexpr int L_END = L_FLG + 24;
constexpr int POLLW = 7;

__global__ void __launch_bounds__(512, 2) __attribute__((aligned(4096))) k_fused(Params P) {
  __shared__ __attribute__((aligned(16))) float lds[L_END];
  volatile LAS unsigned* flg = (volatile LAS unsigned*)(lds + L_FLG);
  const u64x8 ka = *(const CAS u64x8*)__builtin_amdgcn_kernarg_segment_ptr();
  SB();
  const int tid = threadIdx.x, lane = tid & 63, w = __builtin_amdgcn_readfirstlane(tid >> 6);
  const int r = lane >> 4, c = lane & 15;
  const int bid = blockIdx.x, xcd = bid & 7, jj = bid >> 3, hp = xcd * 8 + (jj >> 2);
  const int br = hp >> 5, h = hp & 31, b = (jj >> 1) & 1, sp = jj & 1;
  const int grp = br * 2 + b, pidx = h * 2 + sp;
  constexpr float L2E = 1.4426950408889634f;
  unsigned kvoff = (((b * NH + h) * SK + (SK - WIN) + sp * 1024 + w * 16) * DH) * 2;
  unsigned wqoff = (64 * w * HD + h * DH + 32 * (jj & 3)) * 2, xoff = 64 * w * 2;
  int lo = lane * 16;
  int qo = 4 * (lane >> 2) * (HD * 2) + (lane & 3) * 16;
  const int oo = 8 * r * (DM * 2) + c * 8;
  if (lane < 3) flg[lane * 8 + w] = 0u;
  asm volatile("" : "+s"(kvoff), "+s"(wqoff), "+s"(xoff));
  asm volatile("" : "+v"(lo), "+v"(qo));
  SB();
  const int kvbytes = 1024 * 256 - w * 4096;
  const rsrc_t wqr = mkr((const void*)((br ? ka[3] : ka[2]) + wqoff), 63 * HD * 2 + 64);
  rsrc_t kr, vr, wor;
#define KVOFF(n) (((n) >> 2) * 32768 + ((n) & 3) * 1024)
#define LDK(n) ldb<NT>(kr, lo, KVOFF(n))
#define LDV(n) ldb<NT>(vr, lo, KVOFF(n))
#define LDQ(n) ldb<NT>(wqr, qo, (n) * (HD * 2))
#define LDO(n) ldb2<NT>(wor, oo, (((n) >> 3) * 32 + ((n) & 7)) * (DM * 2))

  v4u A[4], K[32], V[32];
  v2u O[32];
  asm volatile("" :: "s"(wqr));
  SB();
  const unsigned long long t0l = __builtin_amdgcn_s_memrealtime();
  SB();
  sfor<0, 4>([&](auto J) { A[J.v] = LDQ(J.v); SB(); });
  kr = mkr((const void*)((br ? ka[1] : ka[0]) + kvoff), kvbytes);
  sfor<K0, DINIT>([&](auto I) { K[I.v - K0] = LDK(I.v - K0); SB(); });
  const rsrc_t xr = mkr((const void*)((br ? ka[5] : ka[4]) + xoff), DM * 2 + 128);
  const rsrc_t wsr = mkr((const void*)ka[6], WS_END);
  const v2u x0 = ldb2<NT>(xr, 8 * (lane >> 2), 0), x1 = ldb2<NT>(xr, 8 * (lane >> 2), DM * 2);
  const v2u epv = ldb2<SC1 | NT>(wsr, 0, WS_EPOCH + 256 * br);
  BAR();
  asm volatile("" :: "s"(t0l));
  const unsigned t0 = (unsigned)t0l;
  {
    const u64x4 kb = *((const CAS u64x4*)__builtin_amdgcn_kernarg_segment_ptr() + 2);
    vr = mkr((const void*)((br ? kb[1] : kb[0]) + kvoff), kvbytes);
    wor = mkr((const void*)((br ? kb[3] : kb[2]) + (h * DH * DM + 64 * w) * 2), 127 * DM * 2 + 128);
  }

  v4u q;
  float x[16], s[2], p[2], acc[8], m, lam;
  v2u qg;
  v8u WB[4][2];
  unsigned tg0, tg1;
  LAS unsigned* wrp; const LAS v4u* rdp; int yso, ysb, glo, sidx; unsigned tgb;
  unsigned short* outp = (unsigned short*)ka[7] + b * 1024 + br * 512 + (pidx >> 3) * 64 + (pidx & 1) * 32 + ((pidx >> 1) & 3);
#pragma unroll
  for (int i = 0; i < 8; ++i) acc[i] = 0.f;

  auto QK = [&](auto I) {
    constexpr int n = I.v;
    float d = 0.f;
#pragma unroll
    for (int k = 0; k < 4; ++k) d = dot2(K[n][k], q[k], d);
    x[n & 15] = d;
    PIN(x[n & 15]);
  };
  auto TR = [&](int hf) {
#define TR_STEP(HALF, CTRL, BIT) _Pragma("unroll") for (int hh = 0; hh < (HALF); ++hh) { \
      const float lo_ = x[hh], hi_ = x[hh + (HALF)]; const float t1_ = lo_ + dpp<CTRL>(lo_), t2_ = hi_ + dpp<CTRL>(hi_); \
      x[hh] = (c & (BIT)) ? t2_ : t1_; }
    TR_STEP(8, XOR8, 8) TR_STEP(4, XOR7, 4) TR_STEP(2, XOR2, 2) TR_STEP(1, XOR1, 1)
#undef TR_STEP
    s[hf] = rbf(rbf(x[0]) * 0.08838834764831845f);
    PIN(s[hf]);
  };
  auto PV = [&](auto I) {
    constexpr int j = I.v;
    const float pj = dpp<0x150 + (j & 15)>(p[j >> 4]);
#pragma unroll
    for (int k = 0; k < 4; ++k) { acc[2 * k] = fmaf(pj, lo16(V[j][k]), acc[2 * k]); acc[2 * k + 1] = fmaf(pj, hi16(V[j][k]), acc[2 * k + 1]); }
#pragma unroll
    for (int k = 0; k < 8; ++k) PIN(acc[k]);
    SB();
  };
  auto WP = [&](auto I) {
    constexpr int i = I.v;
    WB[0][i >> 3][i & 7] = __builtin_amdgcn_perm(O[2 * i + 1][0], O[2 * i][0], 0x05040100u);
    WB[1][i >> 3][i & 7] = __builtin_amdgcn_perm(O[2 * i + 1][0], O[2 * i][0], 0x07060302u);
    WB[2][i >> 3][i & 7] = __builtin_amdgcn_perm(O[2 * i + 1][1], O[2 * i][1], 0x05040100u);
    WB[3][i >> 3][i & 7] = __builtin_amdgcn_perm(O[2 * i + 1][1], O[2 * i][1], 0x07060302u);
    if constexpr ((i & 7) == 7) { PIN(WB[0][i >> 3]); PIN(WB[1][i >> 3]); PIN(WB[2][i >> 3]); PIN(WB[3][i >> 3]); }
  };

  sfor<DINIT, NSTEP>([&](auto I) {
    constexpr int n = I.v;
    const unsigned tn = pacestep<n>(t0);
    if constexpr (n < V0) K[n - K0] = LDK(n - K0); else V[n - V0] = LDV(n - V0);
    if constexpr (n == QLD) qg = ldb2<SC1 | NT>(wsr, w == 1 ? lane * 8 : OOB, WS_Q + hp * 1024 + b * 512);
    SB();
    sfor<wo_lo(n), wo_lo(n + 1)>([&](auto J) { O[J.v] = LDO(J.v); });
    if constexpr (n == NSTEP - 1)
      __builtin_amdgcn_raw_buffer_store_b128(v4u{__float_as_uint(lam), tg0, tn + t0, tg1}, wsr, tid ? OOB : 0, WS_L + (grp * 64 + pidx) * 16, SC1);
    SB();
    if constexpr (n == QCMP) {
      float qa[16];
#pragma unroll
      for (int k = 0; k < 4; ++k) {
        const unsigned l0 = __builtin_amdgcn_perm(A[1][k], A[0][k], 0x05040100u), h0 = __builtin_amdgcn_perm(A[1][k], A[0][k], 0x07060302u);
        const unsigned l1 = __builtin_amdgcn_perm(A[3][k], A[2][k], 0x05040100u), h1 = __builtin_amdgcn_perm(A[3][k], A[2][k], 0x07060302u);
        qa[2 * k] = dot2(l1, x0[1], dot2(l0, x0[0], 0.f));
        qa[2 * k + 1] = dot2(h1, x0[1], dot2(h0, x0[0], 0.f));
        qa[8 + 2 * k] = dot2(l1, x1[1], dot2(l0, x1[0], 0.f));
        qa[8 + 2 * k + 1] = dot2(h1, x1[1], dot2(h0, x1[0], 0.f));
      }
      float t[8];
#pragma unroll
      for (int i = 0; i < 8; ++i) t[i] = tr32(qa[i], qa[8 + i]);
      f32x4 u;
#pragma unroll
      for (int i = 0; i < 4; ++i) { u[i] = tr16(t[i], t[4 + i]); u[i] += dpp<XOR8>(u[i]); u[i] += dpp<ROR4>(u[i]); }
      if (!(lane & 12)) *(f32x4*)(lds + L_QP + w * 64 + (r >> 1) * 32 + 8 * (lane & 3) + 4 * (r & 1)) = u;
      tg0 = binc(__builtin_amdgcn_readfirstlane(epv[0])); tg1 = binc(__builtin_amdgcn_readfirstlane(epv[1]));
      asm volatile("" : "+s"(outp), "+s"(tg0), "+s"(tg1) :: "memory");
      if (lane == 0) flg[w] = 1u;
    }
    if constexpr (n == QPUB) {
      f32x2 a = {0.f, 0.f};
      if (w == 0) {
        while (!__all(flg[lane & 7] != 0u)) __builtin_amdgcn_s_sleep(1);
        MEMFENCE();
#pragma unroll
        for (int ww = 0; ww < 8; ++ww) a += *(const f32x2*)(lds + L_QP + ww * 64 + 2 * (lane & 31));
      }
      __builtin_amdgcn_raw_buffer_store_b64(v2u{cvtpk(a[0], a[1]), (lane & 1) ? tg1 : tg0}, wsr, tid < 32 ? r * 512 + c * 8 : OOB, WS_Q + hp * 1024 + (jj & 3) * 128, SC1 | NT);
    }
    if constexpr (n == QGOT) {
      if (w == 1) {
        const unsigned want = (lane & 1) ? tg1 : tg0;
        if (__builtin_expect(__builtin_amdgcn_ballot_w64(qg[1] != want) != 0ull, 0)) {
          unsigned spins = 0;
#pragma nounroll
          do {
            __builtin_amdgcn_s_sleep(8); MEMFENCE();
            qg = ldb2<SC1>(wsr, lane * 8, WS_Q + hp * 1024 + b * 512);
          } while (__builtin_amdgcn_ballot_w64(qg[1] != want) != 0ull && ++spins < SPIN_CAP);
        }
        ((unsigned*)lds)[L_Q + lane] = qg[0];
        MEMFENCE();
        if (lane == 0) flg[16] = 1u;
      }
    }
    if constexpr (n == QRD) {
      while (flg[16] == 0u) __builtin_amdgcn_s_sleep(1);
      MEMFENCE();
      q = *(const v4u*)(lds + L_Q + 4 * c);
      PIN(q);
    }
    if constexpr (n > QRD && n <= QRD + 2) sfor<8 * (n - QRD - 1), 8 * (n - QRD - 1) + 8>(QK);
    if constexpr (n == QRD + 3) TR(0);
    if constexpr (n >= SMAX - 3 && n <= SMAX) sfor<16 + 4 * (n - (SMAX - 3)), 20 + 4 * (n - (SMAX - 3))>(QK);
    if constexpr (n == SMAX) {
      TR(1);
      m = fmaxf(s[0], s[1]);
      m = fmaxf(m, dpp<XOR1>(m)); m = fmaxf(m, dpp<XOR2>(m)); m = fmaxf(m, dpp<XOR7>(m)); m = fmaxf(m, dpp<XOR8>(m));
      m = xrow16_max(m);
      p[0] = __builtin_amdgcn_exp2f((s[0] - m) * L2E);
      p[1] = __builtin_amdgcn_exp2f((s[1] - m) * L2E);
      float l = p[0] + p[1];
      l += dpp<XOR1>(l); l += dpp<XOR2>(l); l += dpp<XOR7>(l); l += dpp<XOR8>(l);
      l = tr16(l, l); l = tr32(l, l);
      if (lane == 0) { *(f32x2*)(lds + L_ML + 2 * w) = f32x2{m, l}; MEMFENCE(); flg[8 + w] = 1u; }
    }
    if constexpr (n == FRD) {
      while (!__all(flg[8 + (lane & 7)] != 0u)) __builtin_amdgcn_s_sleep(1);
      MEMFENCE();
      const f32x2 ml = *(const f32x2*)(lds + L_ML + 2 * (lane & 7));
      float M = ml[0];
      M = fmaxf(M, dpp<XOR1>(M)); M = fmaxf(M, dpp<XOR2>(M)); M = fmaxf(M, dpp<XOR7>(M));
      float L = ml[1] * __builtin_amdgcn_exp2f((ml[0] - M) * L2E);
      L += dpp<XOR1>(L); L += dpp<XOR2>(L); L += dpp<XOR7>(L);
      const float f = __builtin_amdgcn_exp2f((m - M) * L2E) / L;
      p[0] *= f; p[1] *= f;
#pragma unroll
      for (int k = 0; k < 8; ++k) { acc[k] *= f; PIN(acc[k]); }
      lam = fmaf(M, L2E, __builtin_amdgcn_logf(L));
      PIN(lam); PIN(p[0]); PIN(p[1]);
    }
    sfor<wo_pairs(n - 1), wo_pairs(n)>(WP);
    if constexpr (n == NSTEP - 2) {
      wrp = (LAS unsigned*)lds + L_OP + (w + 8 * (r & 1)) * OPS + 2 * c + (r >> 1);
      rdp = (const LAS v4u*)((LAS unsigned*)lds + L_OP + c * OPS + 4 * r);
      sidx = (c & 8) ? 0xEEEE : 0x4444;
      yso = lane * 4;
      ysb = WS_Y + (grp * 64 + pidx) * 2048 + 256 * w;
      tgb = (((c & 4) ? tg1 : tg0) >> (8 * (c & 3))) & 0xffu;
      glo = w == POLLW ? lo : OOB;
      asm volatile("" : "+v"(wrp), "+v"(rdp), "+v"(yso), "+v"(tgb), "+v"(glo), "+v"(sidx));
      asm volatile("" : "+s"(ysb));
    }
    SB();
    if constexpr (n > SMAX) sfor<pvdone(n - 1), pvdone(n)>(PV);
    SB();
  });
  f32x4 d[4];
#pragma unroll
  for (int t = 0; t < 4; ++t) { d[t] = f32x4{0.f, 0.f, 0.f, 0.f}; PIN(d[t]); }
  sfor<pvdone(NSTEP - 1), 32>(PV);
  {
    float t[4];
#pragma unroll
    for (int i = 0; i < 4; ++i) t[i] = tr32(acc[i], acc[4 + i]);
    *wrp = cvtpk(tr16(t[0], t[2]), tr16(t[1], t[3]));
  }
  SB();
  v4u gl = ldb<SC1>(wsr, glo, WS_L + grp * 1024);
  BAR();
  {
    const v4u af0 = rdp[0], af1 = rdp[4];
    SB();
#pragma unroll
    for (int t = 0; t < 4; ++t) d[t] = __builtin_amdgcn_smfmac_f32_16x16x64_bf16(__builtin_bit_cast(bf16x8, af0), __builtin_bit_cast(bf16x16, WB[t][0]), d[t], sidx, 0, 0);
    SB();
#pragma unroll
    for (int t = 0; t < 4; ++t) d[t] = __builtin_amdgcn_smfmac_f32_16x16x64_bf16(__builtin_bit_cast(bf16x8, af1), __builtin_bit_cast(bf16x16, WB[t][1]), d[t], sidx, 0, 0);
    SB();
    float e[4];
#pragma unroll
    for (int t = 0; t < 4; ++t) { const f32x2 h2 = f32x2{d[t][0], d[t][1]} + f32x2{d[t][2], d[t][3]}; e[t] = h2[0] + h2[1]; }
    const float y = tr16(tr32(e[0], e[2]), tr32(e[1], e[3]));
    __builtin_amdgcn_raw_buffer_store_b32(gpack(y, tgb), wsr, yso, ysb, SC1);
  }
  if (w == POLLW) {
    const unsigned ts = now();
    unsigned spins = 0;
    if (__builtin_amdgcn_ballot_w64(((gl[1] ^ tg0) | (gl[3] ^ tg1)) != 0u) != 0ull) {
#pragma nounroll
      do {
        MEMFENCE();
        gl = ldb<SC1>(wsr, lo, WS_L + grp * 1024);
      } while (__builtin_amdgcn_ballot_w64(((gl[1] ^ tg0) | (gl[3] ^ tg1)) != 0u) != 0ull && ++spins < SPIN_CAP);
    }
    const float la = __uint_as_float(gl[0]);
    const float wl = __builtin_amdgcn_rcpf(1.0f + __builtin_amdgcn_exp2f(dpp<XOR1>(la) - la));
    float wt[2];
#pragma unroll
    for (int i = 0; i < 2; ++i) wt[i] = __int_as_float(__builtin_amdgcn_ds_bpermute(((lane >> 1) + 32 * i) * 4, __float_as_int(wl)));
    int dl = (int)(gl[2] - ts), dm = dl;
    dl = max(dl, dppi<XOR1>(dl)); dl = max(dl, dppi<XOR2>(dl)); dl = max(dl, dppi<XOR7>(dl)); dl = max(dl, dppi<XOR8>(dl));
    dl = max(max(__builtin_amdgcn_readlane(dl, 0), __builtin_amdgcn_readlane(dl, 16)), max(__builtin_amdgcn_readlane(dl, 32), __builtin_amdgcn_readlane(dl, 48)));
    dm += dppi<XOR1>(dm); dm += dppi<XOR2>(dm); dm += dppi<XOR7>(dm); dm += dppi<XOR8>(dm);
    dm = ((__builtin_amdgcn_readlane(dm, 0) + __builtin_amdgcn_readlane(dm, 16)) + (__builtin_amdgcn_readlane(dm, 32) + __builtin_amdgcn_readlane(dm, 48))) >> 6;
    dl = min(max(max(dl + PLEAD, dm + PLMEAN), PMIN), 400);
    int yo = (lane >> 1) * 2048 + (lane & 1) * 16, yb = WS_Y + grp * 64 * 2048 + pidx * 32, yb1 = yb + 32 * 2048;
    unsigned want = (lane & 1) ? tg1 : tg0;
    asm volatile("" : "+s"(yb), "+s"(yb1));
    const rsrc_t outr = mkr(outp, 58);
    int oso = (lane & 14) ? OOB : (lane & 1) * 32 + (lane >> 4) * 8;
    asm volatile("" : "+v"(yo), "+v"(want), "+v"(oso), "+v"(wt[0]), "+v"(wt[1]));
    while ((int)(now() - ts) < dl) {}
    v4u gy[2];
#pragma nounroll
    for (;;) {
      MEMFENCE();
      gy[0] = ldb<SC1>(wsr, yo, yb); gy[1] = ldb<SC1>(wsr, yo, yb1);
      unsigned bad = 0;
#pragma unroll
      for (int i = 0; i < 2; ++i) bad |= (__builtin_amdgcn_perm(gy[i][1], gy[i][0], 0x0c0c0400u) | __builtin_amdgcn_perm(gy[i][3], gy[i][2], 0x04000c0cu)) ^ want;
      if (__builtin_amdgcn_ballot_w64(bad != 0u) == 0ull) break;
      if (++spins >= SPIN_CAP) break;
    }
    float a[4];
#pragma unroll
    for (int k = 0; k < 4; ++k) a[k] = fmaf(wt[1], gval(gy[1][k]), wt[0] * gval(gy[0][k]));
    float z = tr16(tr32(a[0], a[2]), tr32(a[1], a[3]));
    z += dpp<XOR2>(z); z += dpp<ROR4>(z); z += dpp<XOR8>(z);
    __builtin_amdgcn_raw_buffer_store_b16((unsigned short)cvtpk(z, z), outr, oso, 0, NT);
    if (lane == 0 && pidx == 0 && b == 0) __builtin_amdgcn_raw_buffer_store_b64(v2u{tg0, tg1}, wsr, 0, WS_EPOCH + 256 * br, SC1);
  }
}

extern "C" void kernel_launch(void* const* d_in, const int* in_sizes, int n_in,
                              void* d_out, int out_size, void* d_ws, size_t ws_size,
                              hipStream_t stream) {
  Params P;
  for (int i = 0; i < 2; ++i) {
    P.x[i] = (const unsigned short*)d_in[5 * i + 0];
    P.k[i] = (const unsigned short*)d_in[5 * i + 1];
    P.v[i] = (const unsigned short*)d_in[5 * i + 2];
    P.wq[i] = (const unsigned short*)d_in[5 * i + 3];
    P.wo[i] = (const unsigned short*)d_in[5 * i + 4];
  }
  P.out = (unsigned short*)d_out;
  P.ws = (unsigned char*)d_ws;
  hipLaunchKernelGGL(k_fused, dim3(256), dim3(512), 0, stream, P);
}
```

```cpp
#include <hip/hip_runtime.h>
#include <hip/hip_bf16.h>
#include <stdint.h>

typedef unsigned v4u __attribute__((ext_vector_type(4)));
typedef unsigned v2u __attribute__((ext_vector_type(2)));
typedef unsigned v8u __attribute__((ext_vector_type(8)));
typedef float f32x4 __attribute__((ext_vector_type(4)));
typedef float f32x2 __attribute__((ext_vector_type(2)));
typedef __bf16 bf16x2 __attribute__((ext_vector_type(2)));
typedef __bf16 bf16x8 __attribute__((ext_vector_type(8)));
typedef __bf16 bf16x16 __attribute__((ext_vector_type(16)));
typedef unsigned long long u64x8 __attribute__((ext_vector_type(8)));
typedef unsigned long long u64x4 __attribute__((ext_vector_type(4)));
typedef __amdgpu_buffer_rsrc_t rsrc_t;
#define LAS __attribute__((address_space(3)))
#define CAS __attribute__((address_space(4)))

constexpr int SK = 8192, WIN = 2048, DH = 128, NH = 32, DM = 512, HD = 4096;

constexpr int K0 = 8, V0 = 40, NSTEP = 72;
constexpr int DINIT = 4;
constexpr int DMAX = 5;
constexpr int RATE = 240, OFFS = -400;
constexpr int RATEB = 282, OFFSB = -50;
constexpr int XR4 = 22, GAP = 120;
constexpr int QPUB = 10;
constexpr int QLD = 23;
constexpr int QRD = 31;
constexpr int WO0 = 11, WON = 2, WOD = 3;
constexpr int FRD = 58;
constexpr int PMIN = 12;
constexpr int PLEAD = 118;
constexpr int PLMEAN = 178;
constexpr int SMAX = K0 + 31 + DMAX;
constexpr int clampi(int v, int lo, int hi) { return v < lo ? lo : v > hi ? hi : v; }
constexpr int wo_lo(int n) { return clampi((n - 4) * 2, 0, 8) + clampi(((n - WO0) * WON + WOD - 1) / WOD, 0, 24); }
constexpr int nmain(int n) { return (n >= 4 && n < K0) ? 2 : 1; }
constexpr int nops(int n) { return ((n < 4 || n >= K0) ? 1 : 0) + wo_lo(n + 1) - wo_lo(n) + (n == DINIT - 1 ? 3 : 0) + (n == QPUB) + (n == QLD); }
constexpr int pace(int n, int d) { if (d > n) return 63; int c = -nmain(n - d); for (int i = n - d; i < n; ++i) c += nops(i); return c; }
constexpr int dith(int n) { constexpr int t[4] = {0, 2, 1, 3}; return t[n & 3]; }
constexpr int thr(int n, int d) {
  const int a = OFFS + n * RATE, b = OFFSB + n * RATEB, e = n * XR4;
  return ((d == 3 ? a + ((b - a) * dith(n)) / 3 : d == 4 ? b + (e * dith(n)) / 3 : b + e) + (d - 3) * GAP) / 10;
}
constexpr int wo_pairs(int n) { return wo_lo(n - DMAX) / 2; }
constexpr int QCMP = DINIT + DMAX, QGOT = QLD + DMAX + 1;
constexpr int pvdone(int n) {
  int d = 0;
  for (int i = SMAX + 1; i <= n; ++i) d = clampi(i - DMAX - V0 + 1, 0, d + 2);
  return d;
}
static_assert(QCMP < QPUB && QPUB < QLD && QGOT < QRD && QRD + 3 <= SMAX - 3 && wo_pairs(NSTEP - 2) == 16 && SMAX < FRD && FRD < NSTEP - 2 && WO0 >= K0 && DINIT >= 4 && DINIT <= K0, "schedule");

constexpr int WS_EPOCH = 0;
constexpr int WS_L = 4096;
constexpr int WS_Y = 8192;
constexpr int WS_Q = WS_Y + 256 * 2048;
constexpr int WS_END = WS_Q + 64 * 1024;
constexpr unsigned SPIN_CAP = 1u << 18;
constexpr int OOB = 0x7fffff00;

struct Params {
  const unsigned short* wo[2];
  const unsigned short* wq[2];
  const unsigned short* x[2];
  unsigned char* ws;
  unsigned short* out;
  const unsigned short* v[2];
  const unsigned short* k[2];
};

__device__ __forceinline__ float dot2(unsigned a, unsigned b, float acc) {
  return __builtin_amdgcn_fdot2_f32_bf16(__builtin_bit_cast(bf16x2, a), __builtin_bit_cast(bf16x2, b), acc, false);
}
__device__ __forceinline__ unsigned cvtpk(float lo, float hi) {
  bf16x2 r; r.x = (__bf16)lo; r.y = (__bf16)hi; return __builtin_bit_cast(unsigned, r);
}
__device__ __forceinline__ float rbf(float x) { return (float)(__bf16)x; }
template <int CTRL> __device__ __forceinline__ float dpp(float x) {
  return __builtin_bit_cast(float, __builtin_amdgcn_mov_dpp(__builtin_bit_cast(int, x), CTRL, 0xf, 0xf, true));
}
template <int CTRL> __device__ __forceinline__ int dppi(int x) { return __builtin_amdgcn_mov_dpp(x, CTRL, 0xf, 0xf, true); }
constexpr int XOR1 = 0xB1, XOR2 = 0x4E, XOR7 = 0x141, XOR8 = 0x128, ROR4 = 0x124;
__device__ __forceinline__ float tr32(float a, float b) {
  auto t = __builtin_amdgcn_permlane32_swap(__float_as_uint(a), __float_as_uint(b), false, false);
  return __uint_as_float(t[0]) + __uint_as_float(t[1]);
}
__device__ __forceinline__ float tr16(float a, float b) {
  auto t = __builtin_amdgcn_permlane16_swap(__float_as_uint(a), __float_as_uint(b), false, false);
  return __uint_as_float(t[0]) + __uint_as_float(t[1]);
}
__device__ __forceinline__ float xrow16_max(float x) {
  auto s = __builtin_amdgcn_permlane16_swap(__float_as_uint(x), __float_as_uint(x), false, false);
  x = fmaxf(__uint_as_float(s[0]), __uint_as_float(s[1]));
  auto t = __builtin_amdgcn_permlane32_swap(__float_as_uint(x), __float_as_uint(x), false, false);
  return fmaxf(__uint_as_float(t[0]), __uint_as_float(t[1]));
}
__device__ __forceinline__ float lo16(unsigned u) { return __builtin_bit_cast(float, u << 16); }
__device__ __forceinline__ float hi16(unsigned u) { return __builtin_bit_cast(float, u & 0xffff0000u); }
__device__ __forceinline__ rsrc_t mkr(const void* p, int bytes) { return __builtin_amdgcn_make_buffer_rsrc((void*)p, 0, bytes, 0x00020000); }
template <int AUX> __device__ __forceinline__ v4u ldb(rsrc_t r, int voff, int soff) { return __builtin_amdgcn_raw_buffer_load_b128(r, voff, soff, AUX); }
template <int AUX> __device__ __forceinline__ v2u ldb2(rsrc_t r, int voff, int soff) { return __builtin_amdgcn_raw_buffer_load_b64(r, voff, soff, AUX); }
constexpr int NT = 2, SC1 = 16;
__device__ __forceinline__ unsigned gpack(float v, unsigned tb) { return ((__float_as_uint(v) + 0x80u) & 0xffffff00u) | tb; }
__device__ __forceinline__ float gval(unsigned g) { return __uint_as_float(g & 0xffffff00u); }
__device__ __forceinline__ unsigned binc(unsigned e) { return ((e & 0x7f7f7f7fu) + 0x01010101u) ^ (e & 0x80808080u); }
__device__ __forceinline__ unsigned now() { return (unsigned)__builtin_amdgcn_s_memrealtime(); }

#define SB() __builtin_amdgcn_sched_barrier(0)
#define MEMFENCE() asm volatile("" ::: "memory")
#define PIN(x) asm volatile("" : "+v"(x))
#define BAR() do { SB(); asm volatile("s_waitcnt lgkmcnt(0)" ::: "memory"); __builtin_amdgcn_s_barrier(); asm volatile("" ::: "memory"); SB(); } while (0)
template <int N> struct IC { static constexpr int v = N; };
template <int LO, int HI, class F> __device__ __forceinline__ void sfor(F&& f) { if constexpr (LO < HI) { f(IC<LO>{}); sfor<LO + 1, HI>(f); } }
template <int N> __device__ __forceinline__ unsigned pacestep(unsigned t0) {
  unsigned d;
  asm volatile("s_waitcnt vmcnt(%2)\n\ts_memrealtime vcc\n\ts_waitcnt lgkmcnt(0)\n\ts_sub_i32 %0, vcc_lo, %1\n\t"
               "s_cmpk_ge_i32 %0, %6\n\ts_cbranch_scc1 1f\n\ts_waitcnt vmcnt(%3)\n\t"
               "s_cmpk_ge_i32 %0, %7\n\ts_cbranch_scc1 1f\n\ts_waitcnt vmcnt(%4)\n\t"
               "s_cmpk_ge_i32 %0, %8\n\ts_cbranch_scc1 1f\n\ts_waitcnt vmcnt(%5)\n1:"
               : "=s"(d)
               : "s"(t0), "n"(pace(N, 5)), "n"(pace(N, 4)), "n"(pace(N, 3)), "n"(pace(N, 2)), "n"(thr(N, 5)), "n"(thr(N, 4)), "n"(thr(N, 3))
               : "scc", "vcc");
  SB();
  return d;
}

constexpr int OPS = 36;
constexpr int L_QP = 0;
constexpr int L_OP = 512;
constexpr int L_ML = L_OP + 16 * OPS;
constexpr int L_Q = L_ML + 16;
constexpr int L_FLG = L_Q + 64;
constexpr int L_END = L_FLG + 24;
constexpr int POLLW = 7;

__global__ void __launch_bounds__(512, 2) __attribute__((aligned(4096))) k_fused(Params P) {
  __shared__ __attribute__((aligned(16))) float lds[L_END];
  volatile LAS unsigned* flg = (volatile LAS unsigned*)(lds + L_FLG);
  const u64x8 ka = *(const CAS u64x8*)__builtin_amdgcn_kernarg_segment_ptr();
  SB();
  const int tid = threadIdx.x, lane = tid & 63, w = __builtin_amdgcn_readfirstlane(tid >> 6);
  const int r = lane >> 4, c = lane & 15;
  const int bid = blockIdx.x, xcd = bid & 7, jj = bid >> 3, hp = xcd * 8 + (jj >> 2);
  const int br = hp >> 5, h = hp & 31, b = (jj >> 1) & 1, sp = jj & 1;
  const int grp = br * 2 + b, pidx = h * 2 + sp;
  constexpr float L2E = 1.4426950408889634f;
  unsigned kvoff = (((b * NH + h) * SK + (SK - WIN) + sp * 1024 + w * 16) * DH) * 2;
  unsigned wqoff = (64 * w * HD + h * DH + 32 * (jj & 3)) * 2, xoff = 64 * w * 2;
  int lo = lane * 16;
  int qo = 4 * (lane >> 2) * (HD * 2) + (lane & 3) * 16;
  const int oo = 8 * r * (DM * 2) + c * 8;
  if (lane < 3) flg[lane * 8 + w] = 0u;
  asm volatile("" : "+s"(kvoff), "+s"(wqoff), "+s"(xoff));
  asm volatile("" : "+v"(lo), "+v"(qo));
  SB();
  const int kvbytes = 1024 * 256 - w * 4096;
  const rsrc_t wqr = mkr((const void*)((br ? ka[3] : ka[2]) + wqoff), 63 * HD * 2 + 64);
  rsrc_t kr, vr, wor;
#define KVOFF(n) (((n) >> 2) * 32768 + ((n) & 3) * 1024)
#define LDK(n) ldb<NT>(kr, lo, KVOFF(n))
#define LDV(n) ldb<NT>(vr, lo, KVOFF(n))
#define LDQ(n) ldb<NT>(wqr, qo, (n) * (HD * 2))
#define LDO(n) ldb2<0>(wor, oo, (((((n) >> 3) + jj) & 3) << 15) + ((n) & 7) * (DM * 2))

  v4u A[4], K[32], V[32];
  v2u O[32];
  asm volatile("" :: "s"(wqr));
  SB();
  const unsigned long long t0l = __builtin_amdgcn_s_memrealtime();
  SB();
  sfor<0, 4>([&](auto J) { A[J.v] = LDQ(J.v); SB(); });
  wor = mkr((const void*)((br ? ka[1] : ka[0]) + (h * DH * DM + 64 * w) * 2), 127 * DM * 2 + 128);
  sfor<0, wo_lo(DINIT)>([&](auto J) { O[J.v] = LDO(J.v); SB(); });
  const rsrc_t xr = mkr((const void*)((br ? ka[5] : ka[4]) + xoff), DM * 2 + 128);
  const rsrc_t wsr = mkr((const void*)ka[6], WS_END);
  const v2u x0 = ldb2<NT>(xr, 8 * (lane >> 2), 0), x1 = ldb2<NT>(xr, 8 * (lane >> 2), DM * 2);
  const v2u epv = ldb2<SC1 | NT>(wsr, 0, WS_EPOCH + 256 * br);
  BAR();
  asm volatile("" :: "s"(t0l));
  const unsigned t0 = (unsigned)t0l;
  {
    const u64x4 kb = *((const CAS u64x4*)__builtin_amdgcn_kernarg_segment_ptr() + 2);
    vr = mkr((const void*)((br ? kb[1] : kb[0]) + kvoff), kvbytes);
    kr = mkr((const void*)((br ? kb[3] : kb[2]) + kvoff), kvbytes);
  }

  v4u q;
  float x[16], s[2], p[2], acc[8], m, lam;
  v2u qg;
  v8u WB[4][2];
  unsigned tg0, tg1;
  LAS unsigned* wrp; const LAS v4u* rdp; int yso, ysb, glo, sidx; unsigned tgb;
  unsigned short* outp = (unsigned short*)ka[7] + b * 1024 + br * 512 + (pidx >> 3) * 64 + (pidx & 1) * 32 + ((pidx >> 1) & 3);
#pragma unroll
  for (int i = 0; i < 8; ++i) acc[i] = 0.f;

  auto QK = [&](auto I) {
    constexpr int n = I.v;
    float d = 0.f;
#pragma unroll
    for (int k = 0; k < 4; ++k) d = dot2(K[n][k], q[k], d);
    x[n & 15] = d;
    PIN(x[n & 15]);
  };
  auto TR = [&](int hf) {
#define TR_STEP(HALF, CTRL, BIT) _Pragma("unroll") for (int hh = 0; hh < (HALF); ++hh) { \
      const float lo_ = x[hh], hi_ = x[hh + (HALF)]; const float t1_ = lo_ + dpp<CTRL>(lo_), t2_ = hi_ + dpp<CTRL>(hi_); \
      x[hh] = (c & (BIT)) ? t2_ : t1_; }
    TR_STEP(8, XOR8, 8) TR_STEP(4, XOR7, 4) TR_STEP(2, XOR2, 2) TR_STEP(1, XOR1, 1)
#undef TR_STEP
    s[hf] = rbf(rbf(x[0]) * 0.08838834764831845f);
    PIN(s[hf]);
  };
  auto PV = [&](auto I) {
    constexpr int j = I.v;
    const float pj = dpp<0x150 + (j & 15)>(p[j >> 4]);
#pragma unroll
    for (int k = 0; k < 4; ++k) { acc[2 * k] = fmaf(pj, lo16(V[j][k]), acc[2 * k]); acc[2 * k + 1] = fmaf(pj, hi16(V[j][k]), acc[2 * k + 1]); }
#pragma unroll
    for (int k = 0; k < 8; ++k) PIN(acc[k]);
    SB();
  };
  auto WP = [&](auto I) {
    constexpr int i = I.v;
    WB[0][i >> 3][i & 7] = __builtin_amdgcn_perm(O[2 * i + 1][0], O[2 * i][0], 0x05040100u);
    WB[1][i >> 3][i & 7] = __builtin_amdgcn_perm(O[2 * i + 1][0], O[2 * i][0], 0x07060302u);
    WB[2][i >> 3][i & 7] = __builtin_amdgcn_perm(O[2 * i + 1][1], O[2 * i][1], 0x05040100u);
    WB[3][i >> 3][i & 7] = __builtin_amdgcn_perm(O[2 * i + 1][1], O[2 * i][1], 0x07060302u);
    if constexpr ((i & 7) == 7) { PIN(WB[0][i >> 3]); PIN(WB[1][i >> 3]); PIN(WB[2][i >> 3]); PIN(WB[3][i >> 3]); }
  };

  sfor<DINIT, NSTEP>([&](auto I) {
    constexpr int n = I.v;
    const unsigned tn = pacestep<n>(t0);
    if constexpr (n >= K0 && n < V0) K[n - K0] = LDK(n - K0);
    if constexpr (n >= V0) V[n - V0] = LDV(n - V0);
    if constexpr (n == QLD) qg = ldb2<SC1>(wsr, w == 1 ? lane * 8 : OOB, WS_Q + hp * 1024 + b * 512);
    SB();
    sfor<wo_lo(n), wo_lo(n + 1)>([&](auto J) { O[J.v] = LDO(J.v); });
    if constexpr (n == NSTEP - 1)
      __builtin_amdgcn_raw_buffer_store_b128(v4u{__float_as_uint(lam), tg0, tn + t0, tg1}, wsr, tid ? OOB : 0, WS_L + (grp * 64 + pidx) * 16, SC1);
    SB();
    if constexpr (n == QCMP) {
      float qa[16];
#pragma unroll
      for (int k = 0; k < 4; ++k) {
        const unsigned l0 = __builtin_amdgcn_perm(A[1][k], A[0][k], 0x05040100u), h0 = __builtin_amdgcn_perm(A[1][k], A[0][k], 0x07060302u);
        const unsigned l1 = __builtin_amdgcn_perm(A[3][k], A[2][k], 0x05040100u), h1 = __builtin_amdgcn_perm(A[3][k], A[2][k], 0x07060302u);
        qa[2 * k] = dot2(l1, x0[1], dot2(l0, x0[0], 0.f));
        qa[2 * k + 1] = dot2(h1, x0[1], dot2(h0, x0[0], 0.f));
        qa[8 + 2 * k] = dot2(l1, x1[1], dot2(l0, x1[0], 0.f));
        qa[8 + 2 * k + 1] = dot2(h1, x1[1], dot2(h0, x1[0], 0.f));
      }
      float t[8];
#pragma unroll
      for (int i = 0; i < 8; ++i) t[i] = tr32(qa[i], qa[8 + i]);
      f32x4 u;
#pragma unroll
      for (int i = 0; i < 4; ++i) { u[i] = tr16(t[i], t[4 + i]); u[i] += dpp<XOR8>(u[i]); u[i] += dpp<ROR4>(u[i]); }
      if (!(lane & 12)) *(f32x4*)(lds + L_QP + w * 64 + (r >> 1) * 32 + 8 * (lane & 3) + 4 * (r & 1)) = u;
      tg0 = binc(__builtin_amdgcn_readfirstlane(epv[0])); tg1 = binc(__builtin_amdgcn_readfirstlane(epv[1]));
      asm volatile("" : "+s"(outp), "+s"(tg0), "+s"(tg1) :: "memory");
      if (lane == 0) flg[w] = 1u;
    }
    if constexpr (n == QPUB) {
      f32x2 a = {0.f, 0.f};
      if (w == 0) {
        while (!__all(flg[lane & 7] != 0u)) __builtin_amdgcn_s_sleep(1);
        MEMFENCE();
#pragma unroll
        for (int ww = 0; ww < 8; ++ww) a += *(const f32x2*)(lds + L_QP + ww * 64 + 2 * (lane & 31));
      }
      __builtin_amdgcn_raw_buffer_store_b64(v2u{cvtpk(a[0], a[1]), (lane & 1) ? tg1 : tg0}, wsr, tid < 32 ? r * 512 + c * 8 : OOB, WS_Q + hp * 1024 + (jj & 3) * 128, SC1);
    }
    if constexpr (n == QGOT) {
      if (w == 1) {
        const unsigned want = (lane & 1) ? tg1 : tg0;
        if (__builtin_expect(__builtin_amdgcn_ballot_w64(qg[1] != want) != 0ull, 0)) {
          unsigned spins = 0;
#pragma nounroll
          do {
            __builtin_amdgcn_s_sleep(8); MEMFENCE();
            qg = ldb2<SC1>(wsr, lane * 8, WS_Q + hp * 1024 + b * 512);
          } while (__builtin_amdgcn_ballot_w64(qg[1] != want) != 0ull && ++spins < SPIN_CAP);
        }
        ((unsigned*)lds)[L_Q + lane] = qg[0];
        MEMFENCE();
        if (lane == 0) flg[16] = 1u;
      }
    }
    if constexpr (n == QRD) {
      while (flg[16] == 0u) __builtin_amdgcn_s_sleep(1);
      MEMFENCE();
      q = *(const v4u*)(lds + L_Q + 4 * c);
      PIN(q);
    }
    if constexpr (n > QRD && n <= QRD + 2) sfor<8 * (n - QRD - 1), 8 * (n - QRD - 1) + 8>(QK);
    if constexpr (n == QRD + 3) TR(0);
    if constexpr (n >= SMAX - 3 && n <= SMAX) sfor<16 + 4 * (n - (SMAX - 3)), 20 + 4 * (n - (SMAX - 3))>(QK);
    if constexpr (n == SMAX) {
      TR(1);
      m = fmaxf(s[0], s[1]);
      m = fmaxf(m, dpp<XOR1>(m)); m = fmaxf(m, dpp<XOR2>(m)); m = fmaxf(m, dpp<XOR7>(m)); m = fmaxf(m, dpp<XOR8>(m));
      m = xrow16_max(m);
      p[0] = __builtin_amdgcn_exp2f((s[0] - m) * L2E);
      p[1] = __builtin_amdgcn_exp2f((s[1] - m) * L2E);
      float l = p[0] + p[1];
      l += dpp<XOR1>(l); l += dpp<XOR2>(l); l += dpp<XOR7>(l); l += dpp<XOR8>(l);
      l = tr16(l, l); l = tr32(l, l);
      if (lane == 0) { *(f32x2*)(lds + L_ML + 2 * w) = f32x2{m, l}; MEMFENCE(); flg[8 + w] = 1u; }
    }
    if constexpr (n == FRD) {
      while (!__all(flg[8 + (lane & 7)] != 0u)) __builtin_amdgcn_s_sleep(1);
      MEMFENCE();
      const f32x2 ml = *(const f32x2*)(lds + L_ML + 2 * (lane & 7));
      float M = ml[0];
      M = fmaxf(M, dpp<XOR1>(M)); M = fmaxf(M, dpp<XOR2>(M)); M = fmaxf(M, dpp<XOR7>(M));
      float L = ml[1] * __builtin_amdgcn_exp2f((ml[0] - M) * L2E);
      L += dpp<XOR1>(L); L += dpp<XOR2>(L); L += dpp<XOR7>(L);
      const float f = __builtin_amdgcn_exp2f((m - M) * L2E) / L;
      p[0] *= f; p[1] *= f;
#pragma unroll
      for (int k = 0; k < 8; ++k) { acc[k] *= f; PIN(acc[k]); }
      lam = fmaf(M, L2E, __builtin_amdgcn_logf(L));
      PIN(lam); PIN(p[0]); PIN(p[1]);
    }
    sfor<wo_pairs(n - 1), wo_pairs(n)>(WP);
    if constexpr (n == NSTEP - 2) {
      wrp = (LAS unsigned*)lds + L_OP + (w + 8 * (r & 1)) * OPS + ((2 * c + (r >> 1) - 8 * jj) & 31);
      rdp = (const LAS v4u*)((LAS unsigned*)lds + L_OP + c * OPS + 4 * r);
      sidx = (c & 8) ? 0xEEEE : 0x4444;
      yso = lane * 4;
      ysb = WS_Y + (grp * 64 + pidx) * 2048 + 256 * w;
      tgb = (((c & 4) ? tg1 : tg0) >> (8 * (c & 3))) & 0xffu;
      glo = w == POLLW ? lo : OOB;
      asm volatile("" : "+v"(wrp), "+v"(rdp), "+v"(yso), "+v"(tgb), "+v"(glo), "+v"(sidx));
      asm volatile("" : "+s"(ysb));
    }
    SB();
    if constexpr (n > SMAX) sfor<pvdone(n - 1), pvdone(n)>(PV);
    SB();
  });
  f32x4 d[4];
#pragma unroll
  for (int t = 0; t < 4; ++t) { d[t] = f32x4{0.f, 0.f, 0.f, 0.f}; PIN(d[t]); }
  sfor<pvdone(NSTEP - 1), 32>(PV);
  {
    float t[4];
#pragma unroll
    for (int i = 0; i < 4; ++i) t[i] = tr32(acc[i], acc[4 + i]);
    *wrp = cvtpk(tr16(t[0], t[2]), tr16(t[1], t[3]));
  }
  SB();
  v4u gl = ldb<SC1>(wsr, glo, WS_L + grp * 1024);
  BAR();
  {
    const v4u af0 = rdp[0], af1 = rdp[4];
    SB();
#pragma unroll
    for (int t = 0; t < 4; ++t) d[t] = __builtin_amdgcn_smfmac_f32_16x16x64_bf16(__builtin_bit_cast(bf16x8, af0), __builtin_bit_cast(bf16x16, WB[t][0]), d[t], sidx, 0, 0);
    SB();
#pragma unroll
    for (int t = 0; t < 4; ++t) d[t] = __builtin_amdgcn_smfmac_f32_16x16x64_bf16(__builtin_bit_cast(bf16x8, af1), __builtin_bit_cast(bf16x16, WB[t][1]), d[t], sidx, 0, 0);
    SB();
    float e[4];
#pragma unroll
    for (int t = 0; t < 4; ++t) { const f32x2 h2 = f32x2{d[t][0], d[t][1]} + f32x2{d[t][2], d[t][3]}; e[t] = h2[0] + h2[1]; }
    const float y = tr16(tr32(e[0], e[2]), tr32(e[1], e[3]));
    __builtin_amdgcn_raw_buffer_store_b32(gpack(y, tgb), wsr, yso, ysb, SC1);
  }
  if (w == POLLW) {
    const unsigned ts = now();
    unsigned spins = 0;
    if (__builtin_amdgcn_ballot_w64(((gl[1] ^ tg0) | (gl[3] ^ tg1)) != 0u) != 0ull) {
#pragma nounroll
      do {
        MEMFENCE();
        gl = ldb<SC1>(wsr, lo, WS_L + grp * 1024);
      } while (__builtin_amdgcn_ballot_w64(((gl[1] ^ tg0) | (gl[3] ^ tg1)) != 0u) != 0ull && ++spins < SPIN_CAP);
    }
    const float la = __uint_as_float(gl[0]);
    const float wl = __builtin_amdgcn_rcpf(1.0f + __builtin_amdgcn_exp2f(dpp<XOR1>(la) - la));
    float wt[2];
#pragma unroll
    for (int i = 0; i < 2; ++i) wt[i] = __int_as_float(__builtin_amdgcn_ds_bpermute(((lane >> 1) + 32 * i) * 4, __float_as_int(wl)));
    int dl = (int)(gl[2] - ts), dm = dl;
    dl = max(dl, dppi<XOR1>(dl)); dl = max(dl, dppi<XOR2>(dl)); dl = max(dl, dppi<XOR7>(dl)); dl = max(dl, dppi<XOR8>(dl));
    dl = max(max(__builtin_amdgcn_readlane(dl, 0), __builtin_amdgcn_readlane(dl, 16)), max(__builtin_amdgcn_readlane(dl, 32), __builtin_amdgcn_readlane(dl, 48)));
    dm += dppi<XOR1>(dm); dm += dppi<XOR2>(dm); dm += dppi<XOR7>(dm); dm += dppi<XOR8>(dm);
    dm = ((__builtin_amdgcn_readlane(dm, 0) + __builtin_amdgcn_readlane(dm, 16)) + (__builtin_amdgcn_readlane(dm, 32) + __builtin_amdgcn_readlane(dm, 48))) >> 6;
    dl = min(max(max(dl + PLEAD, dm + PLMEAN), PMIN), 400);
    int yo = (lane >> 1) * 2048 + (lane & 1) * 16, yb = WS_Y + grp * 64 * 2048 + pidx * 32, yb1 = yb + 32 * 2048;
    unsigned want = (lane & 1) ? tg1 : tg0;
    asm volatile("" : "+s"(yb), "+s"(yb1));
    const rsrc_t outr = mkr(outp, 58);
    int oso = (lane & 14) ? OOB : (lane & 1) * 32 + (lane >> 4) * 8;
    asm volatile("" : "+v"(yo), "+v"(want), "+v"(oso), "+v"(wt[0]), "+v"(wt[1]));
    while ((int)(now() - ts) < dl) {}
    v4u gy[2];
#pragma nounroll
    for (;;) {
      MEMFENCE();
      gy[0] = ldb<SC1>(wsr, yo, yb); gy[1] = ldb<SC1>(wsr, yo, yb1);
      unsigned bad = 0;
#pragma unroll
      for (int i = 0; i < 2; ++i) bad |= (__builtin_amdgcn_perm(gy[i][1], gy[i][0], 0x0c0c0400u) | __builtin_amdgcn_perm(gy[i][3], gy[i][2], 0x04000c0cu)) ^ want;
      if (__builtin_amdgcn_ballot_w64(bad != 0u) == 0ull) break;
      if (++spins >= SPIN_CAP) break;
    }
    float a[4];
#pragma unroll
    for (int k = 0; k < 4; ++k) a[k] = fmaf(wt[1], gval(gy[1][k]), wt[0] * gval(gy[0][k]));
    float z = tr16(tr32(a[0], a[2]), tr32(a[1], a[3]));
    z += dpp<XOR2>(z); z += dpp<ROR4>(z); z += dpp<XOR8>(z);
    __builtin_amdgcn_raw_buffer_store_b16((unsigned short)cvtpk(z, z), outr, oso, 0, 0);
    if (lane == 0 && pidx == 0 && b == 0) __builtin_amdgcn_raw_buffer_store_b64(v2u{tg0, tg1}, wsr, 0, WS_EPOCH + 256 * br, SC1);
  }
}

extern "C" void kernel_launch(void* const* d_in, const int* in_sizes, int n_in,
                              void* d_out, int out_size, void* d_ws, size_t ws_size,
                              hipStream_t stream) {
  Params P;
  for (int i = 0; i < 2; ++i) {
    P.x[i] = (const unsigned short*)d_in[5 * i + 0];
    P.k[i] = (const unsigned short*)d_in[5 * i + 1];
    P.v[i] = (const unsigned short*)d_in[5 * i + 2];
    P.wq[i] = (const unsigned short*)d_in[5 * i + 3];
    P.wo[i] = (const unsigned short*)d_in[5 * i + 4];
  }
  P.out = (unsigned short*)d_out;
  P.ws = (unsigned char*)d_ws;
  hipLaunchKernelGGL(k_fused, dim3(256), dim3(512), 0, stream, P);
}
```

```cpp
#include <hip/hip_runtime.h>
#include <hip/hip_bf16.h>
#include <stdint.h>

typedef unsigned v4u __attribute__((ext_vector_type(4)));
typedef unsigned v2u __attribute__((ext_vector_type(2)));
typedef unsigned v8u __attribute__((ext_vector_type(8)));
typedef float f32x4 __attribute__((ext_vector_type(4)));
typedef float f32x2 __attribute__((ext_vector_type(2)));
typedef __bf16 bf16x2 __attribute__((ext_vector_type(2)));
typedef __bf16 bf16x8 __attribute__((ext_vector_type(8)));
typedef __bf16 bf16x16 __attribute__((ext_vector_type(16)));
typedef unsigned long long u64x8 __attribute__((ext_vector_type(8)));
typedef unsigned long long u64x4 __attribute__((ext_vector_type(4)));
typedef __amdgpu_buffer_rsrc_t rsrc_t;
#define LAS __attribute__((address_space(3)))
#define CAS __attribute__((address_space(4)))

constexpr int SK = 8192, WIN = 2048, DH = 128, NH = 32, DM = 512, HD = 4096;

constexpr int K0 = 8, V0 = 40, NSTEP = 72;
constexpr int DINIT = 4;
constexpr int DMAX = 5;
constexpr int RATE = 240, OFFS = -400;
constexpr int RATEB = 282, OFFSB = -50;
constexpr int XR4 = 22, GAP = 120;
constexpr int QPUB = 10;
constexpr int QLD = 23;
constexpr int QRD = 31;
constexpr int WO0 = 11, WON = 2, WOD = 3;
constexpr int FRD = 58;
constexpr int PMIN = 12;
constexpr int PLEAD = 118;
constexpr int PLMEAN = 178;
constexpr int SMAX = K0 + 31 + DMAX;
constexpr int clampi(int v, int lo, int hi) { return v < lo ? lo : v > hi ? hi : v; }
constexpr int wo_lo(int n) { return clampi((n - 4) * 2, 0, 8) + clampi(((n - WO0) * WON + WOD - 1) / WOD, 0, 24); }
constexpr int nmain(int n) { return (n >= 4 && n < K0) ? 2 : 1; }
constexpr int nops(int n) { return ((n < 4 || n >= K0) ? 1 : 0) + wo_lo(n + 1) - wo_lo(n) + (n == DINIT - 1 ? 3 : 0) + (n == QPUB) + (n == QLD); }
constexpr int pace(int n, int d) { if (d > n) return 63; int c = -nmain(n - d); for (int i = n - d; i < n; ++i) c += nops(i); return c; }
constexpr int dith(int n) { constexpr int t[4] = {0, 2, 1, 3}; return t[n & 3]; }
constexpr int thr(int n, int d) {
  const int a = OFFS + n * RATE, b = OFFSB + n * RATEB, e = n * XR4;
  return ((d == 3 ? a + ((b - a) * dith(n)) / 3 : d == 4 ? b + (e * dith(n)) / 3 : b + e) + (d - 3) * GAP) / 10;
}
constexpr int wo_pairs(int n) { return wo_lo(n - DMAX) / 2; }
constexpr int QCMP = DINIT + DMAX, QGOT = QLD + DMAX + 1;
constexpr int pvdone(int n) {
  int d = 0;
  for (int i = SMAX + 1; i <= n; ++i) d = clampi(i - DMAX - V0 + 1, 0, d + 2);
  return d;
}
static_assert(QCMP < QPUB && QPUB < QLD && QGOT < QRD && QRD + 3 <= SMAX - 3 && wo_pairs(NSTEP - 2) == 16 && SMAX < FRD && FRD < NSTEP - 2 && WO0 >= K0 && DINIT >= 4 && DINIT <= K0, "schedule");

constexpr int WS_EPOCH = 0;
constexpr int WS_L = 4096;
constexpr int WS_Y = 8192;
constexpr int WS_Q = WS_Y + 256 * 2048;
constexpr int WS_END = WS_Q + 64 * 1024;
constexpr unsigned SPIN_CAP = 1u << 18;
constexpr int OOB = 0x7fffff00;

struct Params {
  const unsigned short* wo[2];
  const unsigned short* wq[2];
  const unsigned short* x[2];
  unsigned char* ws;
  unsigned short* out;
  const unsigned short* v[2];
  const unsigned short* k[2];
};

__device__ __forceinline__ float dot2(unsigned a, unsigned b, float acc) {
  return __builtin_amdgcn_fdot2_f32_bf16(__builtin_bit_cast(bf16x2, a), __builtin_bit_cast(bf16x2, b), acc, false);
}
__device__ __forceinline__ unsigned cvtpk(float lo, float hi) {
  bf16x2 r; r.x = (__bf16)lo; r.y = (__bf16)hi; return __builtin_bit_cast(unsigned, r);
}
__device__ __forceinline__ float rbf(float x) { return (float)(__bf16)x; }
template <int CTRL> __device__ __forceinline__ float dpp(float x) {
  return __builtin_bit_cast(float, __builtin_amdgcn_mov_dpp(__builtin_bit_cast(int, x), CTRL, 0xf, 0xf, true));
}
template <int CTRL> __device__ __forceinline__ int dppi(int x) { return __builtin_amdgcn_mov_dpp(x, CTRL, 0xf, 0xf, true); }
constexpr int XOR1 = 0xB1, XOR2 = 0x4E, XOR7 = 0x141, XOR8 = 0x128, ROR4 = 0x124;
__device__ __forceinline__ float tr32(float a, float b) {
  auto t = __builtin_amdgcn_permlane32_swap(__float_as_uint(a), __float_as_uint(b), false, false);
  return __uint_as_float(t[0]) + __uint_as_float(t[1]);
}
__device__ __forceinline__ float tr16(float a, float b) {
  auto t = __builtin_amdgcn_permlane16_swap(__float_as_uint(a), __float_as_uint(b), false, false);
  return __uint_as_float(t[0]) + __uint_as_float(t[1]);
}
__device__ __forceinline__ float xrow16_max(float x) {
  auto s = __builtin_amdgcn_permlane16_swap(__float_as_uint(x), __float_as_uint(x), false, false);
  x = fmaxf(__uint_as_float(s[0]), __uint_as_float(s[1]));
  auto t = __builtin_amdgcn_permlane32_swap(__float_as_uint(x), __float_as_uint(x), false, false);
  return fmaxf(__uint_as_float(t[0]), __uint_as_float(t[1]));
}
__device__ __forceinline__ float lo16(unsigned u) { return __builtin_bit_cast(float, u << 16); }
__device__ __forceinline__ float hi16(unsigned u) { return __builtin_bit_cast(float, u & 0xffff0000u); }
__device__ __forceinline__ rsrc_t mkr(const void* p, int bytes) { return __builtin_amdgcn_make_buffer_rsrc((void*)p, 0, bytes, 0x00020000); }
template <int AUX> __device__ __forceinline__ v4u ldb(rsrc_t r, int voff, int soff) { return __builtin_amdgcn_raw_buffer_load_b128(r, voff, soff, AUX); }
template <int AUX> __device__ __forceinline__ v2u ldb2(rsrc_t r, int voff, int soff) { return __builtin_amdgcn_raw_buffer_load_b64(r, voff, soff, AUX); }
constexpr int NT = 2, SC1 = 16;
__device__ __forceinline__ unsigned gpack(float v, unsigned tb) { return ((__float_as_uint(v) + 0x80u) & 0xffffff00u) | tb; }
__device__ __forceinline__ float gval(unsigned g) { return __uint_as_float(g & 0xffffff00u); }
__device__ __forceinline__ unsigned binc(unsigned e) { return ((e & 0x7f7f7f7fu) + 0x01010101u) ^ (e & 0x80808080u); }
__device__ __forceinline__ unsigned now() { return (unsigned)__builtin_amdgcn_s_memrealtime(); }

#define SB() __builtin_amdgcn_sched_barrier(0)
#define MEMFENCE() asm volatile("" ::: "memory")
#define PIN(x) asm volatile("" : "+v"(x))
#define BAR() do { SB(); asm volatile("s_waitcnt lgkmcnt(0)" ::: "memory"); __builtin_amdgcn_s_barrier(); asm volatile("" ::: "memory"); SB(); } while (0)
template <int N> struct IC { static constexpr int v = N; };
template <int LO, int HI, class F> __device__ __forceinline__ void sfor(F&& f) { if constexpr (LO < HI) { f(IC<LO>{}); sfor<LO + 1, HI>(f); } }
template <int N> __device__ __forceinline__ unsigned pacestep(unsigned t0) {
  unsigned d;
  asm volatile("s_waitcnt vmcnt(%2)\n\ts_memrealtime vcc\n\ts_waitcnt lgkmcnt(0)\n\ts_sub_i32 %0, vcc_lo, %1\n\t"
               "s_cmpk_ge_i32 %0, %6\n\ts_cbranch_scc1 1f\n\ts_waitcnt vmcnt(%3)\n\t"
               "s_cmpk_ge_i32 %0, %7\n\ts_cbranch_scc1 1f\n\ts_waitcnt vmcnt(%4)\n\t"
               "s_cmpk_ge_i32 %0, %8\n\ts_cbranch_scc1 1f\n\ts_waitcnt vmcnt(%5)\n1:"
               : "=s"(d)
               : "s"(t0), "n"(pace(N, 5)), "n"(pace(N, 4)), "n"(pace(N, 3)), "n"(pace(N, 2)), "n"(thr(N, 5)), "n"(thr(N, 4)), "n"(thr(N, 3))
               : "scc", "vcc");
  SB();
  return d;
}

constexpr int OPS = 36;
constexpr int L_QP = 0;
constexpr int L_OP = 512;
constexpr int L_ML = L_OP + 16 * OPS;
constexpr int L_Q = L_ML + 16;
constexpr int L_FLG = L_Q + 64;
constexpr int L_END = L_FLG + 24;
constexpr int POLLW = 7;

__global__ void __launch_bounds__(512, 2) __attribute__((aligned(4096))) k_fused(Params P) {
  __shared__ __attribute__((aligned(16))) float lds[L_END];
  volatile LAS unsigned* flg = (volatile LAS unsigned*)(lds + L_FLG);
  const u64x8 ka = *(const CAS u64x8*)__builtin_amdgcn_kernarg_segment_ptr();
  SB();
  const int tid = threadIdx.x, lane = tid & 63, w = __builtin_amdgcn_readfirstlane(tid >> 6);
  const int r = lane >> 4, c = lane & 15;
  const int bid = blockIdx.x, xcd = bid & 7, jj = bid >> 3, hp = xcd * 8 + (jj >> 2);
  const int br = hp >> 5, h = hp & 31, b = (jj >> 1) & 1, sp = jj & 1;
  const int grp = br * 2 + b, pidx = h * 2 + sp;
  constexpr float L2E = 1.4426950408889634f;
  unsigned kvoff = (((b * NH + h) * SK + (SK - WIN) + sp * 1024 + w * 16) * DH) * 2;
  unsigned wqoff = (64 * w * HD + h * DH + 32 * (jj & 3)) * 2, xoff = 64 * w * 2;
  int lo = lane * 16;
  int qo = 4 * (lane >> 2) * (HD * 2) + (lane & 3) * 16;
  const int oo = 8 * r * (DM * 2) + c * 8;
  if (lane < 3) flg[lane * 8 + w] = 0u;
  asm volatile("" : "+s"(kvoff), "+s"(wqoff), "+s"(xoff));
  asm volatile("" : "+v"(lo), "+v"(qo));
  SB();
  const int kvbytes = 1024 * 256 - w * 4096;
  const rsrc_t wqr = mkr((const void*)((br ? ka[3] : ka[2]) + wqoff), 63 * HD * 2 + 64);
  rsrc_t kr, vr, wor;
#define KVOFF(n) (((n) >> 2) * 32768 + ((n) & 3) * 1024)
#define LDK(n) ldb<NT>(kr, lo, KVOFF(n))
#define LDV(n) ldb<NT>(vr, lo, KVOFF(n))
#define LDQ(n) ldb<NT>(wqr, qo, (n) * (HD * 2))
#define LDO(n) ldb2<0>(wor, oo, (((((n) >> 3) + jj) & 3) << 15) + ((n) & 7) * (DM * 2))

  v4u A[4], K[32], V[32];
  v2u O[32];
  asm volatile("" :: "s"(wqr));
  SB();
  const unsigned long long t0l = __builtin_amdgcn_s_memrealtime();
  SB();
  sfor<0, 4>([&](auto J) { A[J.v] = LDQ(J.v); SB(); });
  wor = mkr((const void*)((br ? ka[1] : ka[0]) + (h * DH * DM + 64 * w) * 2), 127 * DM * 2 + 128);
  sfor<0, wo_lo(DINIT)>([&](auto J) { O[J.v] = LDO(J.v); SB(); });
  const rsrc_t xr = mkr((const void*)((br ? ka[5] : ka[4]) + xoff), DM * 2 + 128);
  const rsrc_t wsr = mkr((const void*)ka[6], WS_END);
  const v2u x0 = ldb2<NT>(xr, 8 * (lane >> 2), 0), x1 = ldb2<NT>(xr, 8 * (lane >> 2), DM * 2);
  const v2u epv = ldb2<SC1 | NT>(wsr, 0, WS_EPOCH + 256 * br);
  BAR();
  asm volatile("" :: "s"(t0l));
  const unsigned t0 = (unsigned)t0l;
  {
    const u64x4 kb = *((const CAS u64x4*)__builtin_amdgcn_kernarg_segment_ptr() + 2);
    vr = mkr((const void*)((br ? kb[1] : kb[0]) + kvoff), kvbytes);
    kr = mkr((const void*)((br ? kb[3] : kb[2]) + kvoff), kvbytes);
  }

  v4u q;
  float x[16], s[2], p[2], acc[8], m, lam;
  v2u qg;
  v8u WB[4][2];
  unsigned tg0, tg1;
  LAS unsigned* wrp; const LAS v4u* rdp; int yso, ysb, glo, sidx; unsigned tgb;
  unsigned short* outp = (unsigned short*)ka[7] + b * 1024 + br * 512 + (pidx >> 3) * 64 + (pidx & 1) * 32 + ((pidx >> 1) & 3);
#pragma unroll
  for (int i = 0; i < 8; ++i) acc[i] = 0.f;

  auto QK = [&](auto I) {
    constexpr int n = I.v;
    float d = 0.f;
#pragma unroll
    for (int k = 0; k < 4; ++k) d = dot2(K[n][k], q[k], d);
    x[n & 15] = d;
    PIN(x[n & 15]);
  };
  auto TR = [&](int hf) {
#define TR_STEP(HALF, CTRL, BIT) _Pragma("unroll") for (int hh = 0; hh < (HALF); ++hh) { \
      const float lo_ = x[hh], hi_ = x[hh + (HALF)]; const float t1_ = lo_ + dpp<CTRL>(lo_), t2_ = hi_ + dpp<CTRL>(hi_); \
      x[hh] = (c & (BIT)) ? t2_ : t1_; }
    TR_STEP(8, XOR8, 8) TR_STEP(4, XOR7, 4) TR_STEP(2, XOR2, 2) TR_STEP(1, XOR1, 1)
#undef TR_STEP
    s[hf] = rbf(rbf(x[0]) * 0.08838834764831845f);
    PIN(s[hf]);
  };
  auto PV = [&](auto I) {
    constexpr int j = I.v;
    const float pj = dpp<0x150 + (j & 15)>(p[j >> 4]);
#pragma unroll
    for (int k = 0; k < 4; ++k) { acc[2 * k] = fmaf(pj, lo16(V[j][k]), acc[2 * k]); acc[2 * k + 1] = fmaf(pj, hi16(V[j][k]), acc[2 * k + 1]); }
#pragma unroll
    for (int k = 0; k < 8; ++k) PIN(acc[k]);
    SB();
  };
  auto WP = [&](auto I) {
    constexpr int i = I.v;
    WB[0][i >> 3][i & 7] = __builtin_amdgcn_perm(O[2 * i + 1][0], O[2 * i][0], 0x05040100u);
    WB[1][i >> 3][i & 7] = __builtin_amdgcn_perm(O[2 * i + 1][0], O[2 * i][0], 0x07060302u);
    WB[2][i >> 3][i & 7] = __builtin_amdgcn_perm(O[2 * i + 1][1], O[2 * i][1], 0x05040100u);
    WB[3][i >> 3][i & 7] = __builtin_amdgcn_perm(O[2 * i + 1][1], O[2 * i][1], 0x07060302u);
    if constexpr ((i & 7) == 7) { PIN(WB[0][i >> 3]); PIN(WB[1][i >> 3]); PIN(WB[2][i >> 3]); PIN(WB[3][i >> 3]); }
  };

  sfor<DINIT, NSTEP>([&](auto I) {
    constexpr int n = I.v;
    const unsigned tn = pacestep<n>(t0);
    if constexpr (n >= K0 && n < V0) K[n - K0] = LDK(n - K0);
    if constexpr (n >= V0) V[n - V0] = LDV(n - V0);
    if constexpr (n == QLD) qg = ldb2<SC1>(wsr, w == 1 ? lane * 8 : OOB, WS_Q + hp * 1024 + b * 512);
    SB();
    sfor<wo_lo(n), wo_lo(n + 1)>([&](auto J) { O[J.v] = LDO(J.v); });
    if constexpr (n == NSTEP - 1)
      __builtin_amdgcn_raw_buffer_store_b128(v4u{__float_as_uint(lam), tg0, tn + t0, tg1}, wsr, tid ? OOB : 0, WS_L + (grp * 64 + pidx) * 16, SC1);
    SB();
    if constexpr (n == QCMP) {
      float qa[16];
#pragma unroll
      for (int k = 0; k < 4; ++k) {
        const unsigned l0 = __builtin_amdgcn_perm(A[1][k], A[0][k], 0x05040100u), h0 = __builtin_amdgcn_perm(A[1][k], A[0][k], 0x07060302u);
        const unsigned l1 = __builtin_amdgcn_perm(A[3][k], A[2][k], 0x05040100u), h1 = __builtin_amdgcn_perm(A[3][k], A[2][k], 0x07060302u);
        qa[2 * k] = dot2(l1, x0[1], dot2(l0, x0[0], 0.f));
        qa[2 * k + 1] = dot2(h1, x0[1], dot2(h0, x0[0], 0.f));
        qa[8 + 2 * k] = dot2(l1, x1[1], dot2(l0, x1[0], 0.f));
        qa[8 + 2 * k + 1] = dot2(h1, x1[1], dot2(h0, x1[0], 0.f));
      }
      float t[8];
#pragma unroll
      for (int i = 0; i < 8; ++i) t[i] = tr32(qa[i], qa[8 + i]);
      f32x4 u;
#pragma unroll
      for (int i = 0; i < 4; ++i) { u[i] = tr16(t[i], t[4 + i]); u[i] += dpp<XOR8>(u[i]); u[i] += dpp<ROR4>(u[i]); }
      if (!(lane & 12)) *(f32x4*)(lds + L_QP + w * 64 + (r >> 1) * 32 + 8 * (lane & 3) + 4 * (r & 1)) = u;
      tg0 = binc(__builtin_amdgcn_readfirstlane(epv[0])); tg1 = binc(__builtin_amdgcn_readfirstlane(epv[1]));
      asm volatile("" : "+s"(outp), "+s"(tg0), "+s"(tg1) :: "memory");
      if (lane == 0) flg[w] = 1u;
    }
    if constexpr (n == QPUB) {
      f32x2 a = {0.f, 0.f};
      if (w == 0) {
        while (!__all(flg[lane & 7] != 0u)) __builtin_amdgcn_s_sleep(1);
        MEMFENCE();
#pragma unroll
        for (int ww = 0; ww < 8; ++ww) a += *(const f32x2*)(lds + L_QP + ww * 64 + 2 * (lane & 31));
      }
      __builtin_amdgcn_raw_buffer_store_b64(v2u{cvtpk(a[0], a[1]), (lane & 1) ? tg1 : tg0}, wsr, tid < 32 ? r * 512 + c * 8 : OOB, WS_Q + hp * 1024 + (jj & 3) * 128, SC1);
    }
    if constexpr (n == QGOT) {
      if (w == 1) {
        const unsigned want = (lane & 1) ? tg1 : tg0;
        if (__builtin_expect(__builtin_amdgcn_ballot_w64(qg[1] != want) != 0ull, 0)) {
          unsigned spins = 0;
#pragma nounroll
          do {
            __builtin_amdgcn_s_sleep(8); MEMFENCE();
            qg = ldb2<SC1>(wsr, lane * 8, WS_Q + hp * 1024 + b * 512);
          } while (__builtin_amdgcn_ballot_w64(qg[1] != want) != 0ull && ++spins < SPIN_CAP);
        }
        ((unsigned*)lds)[L_Q + lane] = qg[0];
        MEMFENCE();
        if (lane == 0) flg[16] = 1u;
      }
    }
    if constexpr (n == QRD) {
      while (flg[16] == 0u) __builtin_amdgcn_s_sleep(1);
      MEMFENCE();
      q = *(const v4u*)(lds + L_Q + 4 * c);
      PIN(q);
    }
    if constexpr (n > QRD && n <= QRD + 2) sfor<8 * (n - QRD - 1), 8 * (n - QRD - 1) + 8>(QK);
    if constexpr (n == QRD + 3) TR(0);
    if constexpr (n >= SMAX - 3 && n <= SMAX) sfor<16 + 4 * (n - (SMAX - 3)), 20 + 4 * (n - (SMAX - 3))>(QK);
    if constexpr (n == SMAX) {
      TR(1);
      m = fmaxf(s[0], s[1]);
      m = fmaxf(m, dpp<XOR1>(m)); m = fmaxf(m, dpp<XOR2>(m)); m = fmaxf(m, dpp<XOR7>(m)); m = fmaxf(m, dpp<XOR8>(m));
      m = xrow16_max(m);
      p[0] = __builtin_amdgcn_exp2f((s[0] - m) * L2E);
      p[1] = __builtin_amdgcn_exp2f((s[1] - m) * L2E);
      float l = p[0] + p[1];
      l += dpp<XOR1>(l); l += dpp<XOR2>(l); l += dpp<XOR7>(l); l += dpp<XOR8>(l);
      l = tr16(l, l); l = tr32(l, l);
      if (lane == 0) { *(f32x2*)(lds + L_ML + 2 * w) = f32x2{m, l}; MEMFENCE(); flg[8 + w] = 1u; }
    }
    if constexpr (n == FRD) {
      while (!__all(flg[8 + (lane & 7)] != 0u)) __builtin_amdgcn_s_sleep(1);
      MEMFENCE();
      const f32x2 ml = *(const f32x2*)(lds + L_ML + 2 * (lane & 7));
      float M = ml[0];
      M = fmaxf(M, dpp<XOR1>(M)); M = fmaxf(M, dpp<XOR2>(M)); M = fmaxf(M, dpp<XOR7>(M));
      float L = ml[1] * __builtin_amdgcn_exp2f((ml[0] - M) * L2E);
      L += dpp<XOR1>(L); L += dpp<XOR2>(L); L += dpp<XOR7>(L);
      const float f = __builtin_amdgcn_exp2f((m - M) * L2E) / L;
      p[0] *= f; p[1] *= f;
#pragma unroll
      for (int k = 0; k < 8; ++k) { acc[k] *= f; PIN(acc[k]); }
      lam = fmaf(M, L2E, __builtin_amdgcn_logf(L));
      PIN(lam); PIN(p[0]); PIN(p[1]);
    }
    sfor<wo_pairs(n - 1), wo_pairs(n)>(WP);
    if constexpr (n == NSTEP - 2) {
      wrp = (LAS unsigned*)lds + L_OP + (w + 8 * (r & 1)) * OPS + ((2 * c + (r >> 1) - 8 * jj) & 31);
      rdp = (const LAS v4u*)((LAS unsigned*)lds + L_OP + c * OPS + 4 * r);
      sidx = (c & 8) ? 0xEEEE : 0x4444;
      yso = lane * 4;
      ysb = WS_Y + (grp * 64 + pidx) * 2048 + 256 * w;
      tgb = (((c & 4) ? tg1 : tg0) >> (8 * (c & 3))) & 0xffu;
      glo = w == POLLW ? lo : OOB;
      asm volatile("" : "+v"(wrp), "+v"(rdp), "+v"(yso), "+v"(tgb), "+v"(glo), "+v"(sidx));
      asm volatile("" : "+s"(ysb));
    }
    SB();
    if constexpr (n > SMAX) sfor<pvdone(n - 1), pvdone(n)>(PV);
    SB();
  });
  f32x4 d[4];
#pragma unroll
  for (int t = 0; t < 4; ++t) { d[t] = f32x4{0.f, 0.f, 0.f, 0.f}; PIN(d[t]); }
  sfor<pvdone(NSTEP - 1), 32>(PV);
  {
    float t[4];
#pragma unroll
    for (int i = 0; i < 4; ++i) t[i] = tr32(acc[i], acc[4 + i]);
    *wrp = cvtpk(tr16(t[0], t[2]), tr16(t[1], t[3]));
  }
  SB();
  v4u gl = ldb<SC1>(wsr, glo, WS_L + grp * 1024);
  BAR();
  {
    const v4u af0 = rdp[0], af1 = rdp[4];
    SB();
#pragma unroll
    for (int t = 0; t < 4; ++t) d[t] = __builtin_amdgcn_smfmac_f32_16x16x64_bf16(__builtin_bit_cast(bf16x8, af0), __builtin_bit_cast(bf16x16, WB[t][0]), d[t], sidx, 0, 0);
    SB();
#pragma unroll
    for (int t = 0; t < 4; ++t) d[t] = __builtin_amdgcn_smfmac_f32_16x16x64_bf16(__builtin_bit_cast(bf16x8, af1), __builtin_bit_cast(bf16x16, WB[t][1]), d[t], sidx, 0, 0);
    SB();
    float e[4];
#pragma unroll
    for (int t = 0; t < 4; ++t) { const f32x2 h2 = f32x2{d[t][0], d[t][1]} + f32x2{d[t][2], d[t][3]}; e[t] = h2[0] + h2[1]; }
    const float y = tr16(tr32(e[0], e[2]), tr32(e[1], e[3]));
    __builtin_amdgcn_raw_buffer_store_b32(gpack(y, tgb), wsr, yso, ysb, SC1);
  }
  if (w == POLLW) {
    const unsigned ts = now();
    unsigned spins = 0;
    if (__builtin_amdgcn_ballot_w64(((gl[1] ^ tg0) | (gl[3] ^ tg1)) != 0u) != 0ull) {
#pragma nounroll
      do {
        MEMFENCE();
        gl = ldb<SC1>(wsr, lo, WS_L + grp * 1024);
      } while (__builtin_amdgcn_ballot_w64(((gl[1] ^ tg0) | (gl[3] ^ tg1)) != 0u) != 0ull && ++spins < SPIN_CAP);
    }
    const float la = __uint_as_float(gl[0]);
    const float wl = __builtin_amdgcn_rcpf(1.0f + __builtin_amdgcn_exp2f(dpp<XOR1>(la) - la));
    float wt[2];
#pragma unroll
    for (int i = 0; i < 2; ++i) wt[i] = __int_as_float(__builtin_amdgcn_ds_bpermute(((lane >> 1) + 32 * i) * 4, __float_as_int(wl)));
    int dl = (int)(gl[2] - ts), dm = dl;
    dl = max(dl, dppi<XOR1>(dl)); dl = max(dl, dppi<XOR2>(dl)); dl = max(dl, dppi<XOR7>(dl)); dl = max(dl, dppi<XOR8>(dl));
    dl = max(max(__builtin_amdgcn_readlane(dl, 0), __builtin_amdgcn_readlane(dl, 16)), max(__builtin_amdgcn_readlane(dl, 32), __builtin_amdgcn_readlane(dl, 48)));
    dm += dppi<XOR1>(dm); dm += dppi<XOR2>(dm); dm += dppi<XOR7>(dm); dm += dppi<XOR8>(dm);
    dm = ((__builtin_amdgcn_readlane(dm, 0) + __builtin_amdgcn_readlane(dm, 16)) + (__builtin_amdgcn_readlane(dm, 32) + __builtin_amdgcn_readlane(dm, 48))) >> 6;
    dl = min(max(max(dl + PLEAD, dm + PLMEAN), PMIN), 400);
    int yo = (lane >> 1) * 2048 + (lane & 1) * 16, yb = WS_Y + grp * 64 * 2048 + pidx * 32, yb1 = yb + 32 * 2048;
    unsigned want = (lane & 1) ? tg1 : tg0;
    asm volatile("" : "+s"(yb), "+s"(yb1));
    const rsrc_t outr = mkr(outp, 58);
    int oso = (lane & 14) ? OOB : (lane & 1) * 32 + (lane >> 4) * 8;
    asm volatile("" : "+v"(yo), "+v"(want), "+v"(oso), "+v"(wt[0]), "+v"(wt[1]));
    while ((int)(now() - ts) < dl) {}
#define YBAD ((((__builtin_amdgcn_perm(gy0[1], gy0[0], 0x0c0c0400u) | __builtin_amdgcn_perm(gy0[3], gy0[2], 0x04000c0cu)) ^ want) | \
              ((__builtin_amdgcn_perm(gy1[1], gy1[0], 0x0c0c0400u) | __builtin_amdgcn_perm(gy1[3], gy1[2], 0x04000c0cu)) ^ want)) != 0u)
    MEMFENCE();
    v4u gy0 = ldb<SC1>(wsr, yo, yb), gy1 = ldb<SC1>(wsr, yo, yb1);
    if (__builtin_expect(__builtin_amdgcn_ballot_w64(YBAD) != 0ull, 0)) {
#pragma nounroll
      do {
        MEMFENCE();
        gy0 = ldb<SC1>(wsr, yo, yb); gy1 = ldb<SC1>(wsr, yo, yb1);
      } while (__builtin_amdgcn_ballot_w64(YBAD) != 0ull && ++spins < SPIN_CAP);
    }
#undef YBAD
    const f32x2 a01 = __builtin_elementwise_fma(f32x2{wt[1], wt[1]}, f32x2{gval(gy1[0]), gval(gy1[1])}, wt[0] * f32x2{gval(gy0[0]), gval(gy0[1])});
    const f32x2 a23 = __builtin_elementwise_fma(f32x2{wt[1], wt[1]}, f32x2{gval(gy1[2]), gval(gy1[3])}, wt[0] * f32x2{gval(gy0[2]), gval(gy0[3])});
    const float a[4] = {a01[0], a01[1], a23[0], a23[1]};
    float z = tr16(tr32(a[0], a[2]), tr32(a[1], a[3]));
    z += dpp<XOR2>(z); z += dpp<ROR4>(z); z += dpp<XOR8>(z);
    __builtin_amdgcn_raw_buffer_store_b16((unsigned short)cvtpk(z, z), outr, oso, 0, 0);
    if (lane == 0 && pidx == 0 && b == 0) __builtin_amdgcn_raw_buffer_store_b64(v2u{tg0, tg1}, wsr, 0, WS_EPOCH + 256 * br, SC1);
  }
}

extern "C" void kernel_launch(void* const* d_in, const int* in_sizes, int n_in,
                              void* d_out, int out_size, void* d_ws, size_t ws_size,
                              hipStream_t stream) {
  Params P;
  for (int i = 0; i < 2; ++i) {
    P.x[i] = (const unsigned short*)d_in[5 * i + 0];
    P.k[i] = (const unsigned short*)d_in[5 * i + 1];
    P.v[i] = (const unsigned short*)d_in[5 * i + 2];
    P.wq[i] = (const unsigned short*)d_in[5 * i + 3];
    P.wo[i] = (const unsigned short*)d_in[5 * i + 4];
  }
  P.out = (unsigned short*)d_out;
  P.ws = (unsigned char*)d_ws;
  hipLaunchKernelGGL(k_fused, dim3(256), dim3(512), 0, stream, P);
}
```

```cpp
#include <hip/hip_runtime.h>
#include <hip/hip_bf16.h>
#include <stdint.h>

typedef unsigned v4u __attribute__((ext_vector_type(4)));
typedef unsigned v2u __attribute__((ext_vector_type(2)));
typedef unsigned v8u __attribute__((ext_vector_type(8)));
typedef float f32x4 __attribute__((ext_vector_type(4)));
typedef float f32x2 __attribute__((ext_vector_type(2)));
typedef __bf16 bf16x2 __attribute__((ext_vector_type(2)));
typedef __bf16 bf16x8 __attribute__((ext_vector_type(8)));
typedef __bf16 bf16x16 __attribute__((ext_vector_type(16)));
typedef unsigned long long u64x8 __attribute__((ext_vector_type(8)));
typedef unsigned long long u64x4 __attribute__((ext_vector_type(4)));
typedef __amdgpu_buffer_rsrc_t rsrc_t;
#define LAS __attribute__((address_space(3)))
#define CAS __attribute__((address_space(4)))

constexpr int SK = 8192, WIN = 2048, DH = 128, NH = 32, DM = 512, HD = 4096;

constexpr int K0 = 8, V0 = 40, NSTEP = 72;
constexpr int DINIT = 4;
constexpr int DMAX = 5;
constexpr int RATE = 240, OFFS = -550;
constexpr int RATEB = 282, OFFSB = -200;
constexpr int XR4 = 22, GAP = 120;
constexpr int XS = 9;
constexpr int QPUB = 15;
constexpr int QLD = 23;
constexpr int QRD = 31;
constexpr int WO0 = 11, WON = 2, WOD = 3;
constexpr int FRD = 58;
constexpr int PMIN = 12;
constexpr int PLEAD = 118;
constexpr int PLMEAN = 178;
constexpr int SMAX = K0 + 31 + DMAX;
constexpr int clampi(int v, int lo, int hi) { return v < lo ? lo : v > hi ? hi : v; }
constexpr int wo_lo(int n) { return clampi((n - 4) * 2, 0, 8) + clampi(((n - WO0) * WON + WOD - 1) / WOD, 0, 24); }
constexpr int nmain(int n) { return (n >= 4 && n < K0) ? 2 : 1; }
constexpr int nops(int n) { return ((n < 4 || n >= K0) ? 1 : 0) + wo_lo(n + 1) - wo_lo(n) + (n == XS ? 3 : 0) + (n == QPUB) + (n == QLD); }
constexpr int pace(int n, int d) { if (d > n) return 63; int c = -nmain(n - d); for (int i = n - d; i < n; ++i) c += nops(i); return c; }
constexpr int dith(int n) { constexpr int t[4] = {0, 2, 1, 3}; return t[n & 3]; }
constexpr int thr(int n, int d) {
  const int a = OFFS + n * RATE, b = OFFSB + n * RATEB, e = n * XR4;
  return ((d == 3 ? a + ((b - a) * dith(n)) / 3 : d == 4 ? b + (e * dith(n)) / 3 : b + e) + (d - 3) * GAP) / 10;
}
constexpr int wo_pairs(int n) { return wo_lo(n - DMAX) / 2; }
constexpr int QCMP = XS + 4, QGOT = QLD + DMAX + 1;
constexpr int pvdone(int n) {
  int d = 0;
  for (int i = SMAX + 1; i <= n; ++i) d = clampi(i - DMAX - V0 + 1, 0, d + 2);
  return d;
}
static_assert(QCMP < QPUB && QPUB < QLD && QGOT < QRD && QRD + 3 <= SMAX - 3 && wo_pairs(NSTEP - 2) == 16 && SMAX < FRD && FRD < NSTEP - 2 && WO0 >= K0 && DINIT == 4 && XS >= DINIT, "schedule");

constexpr int WS_EPOCH = 0;
constexpr int WS_L = 4096;
constexpr int WS_Y = 8192;
constexpr int WS_Q = WS_Y + 256 * 2048;
constexpr int WS_END = WS_Q + 64 * 1024;
constexpr unsigned SPIN_CAP = 1u << 18;
constexpr int OOB = 0x7fffff00;

struct Params {
  const unsigned short* wo[2];
  const unsigned short* wq[2];
  const unsigned short* x[2];
  unsigned char* ws;
  unsigned short* out;
  const unsigned short* v[2];
  const unsigned short* k[2];
};

__device__ __forceinline__ float dot2(unsigned a, unsigned b, float acc) {
  return __builtin_amdgcn_fdot2_f32_bf16(__builtin_bit_cast(bf16x2, a), __builtin_bit_cast(bf16x2, b), acc, false);
}
__device__ __forceinline__ unsigned cvtpk(float lo, float hi) {
  bf16x2 r; r.x = (__bf16)lo; r.y = (__bf16)hi; return __builtin_bit_cast(unsigned, r);
}
__device__ __forceinline__ float rbf(float x) { return (float)(__bf16)x; }
template <int CTRL> __device__ __forceinline__ float dpp(float x) {
  return __builtin_bit_cast(float, __builtin_amdgcn_mov_dpp(__builtin_bit_cast(int, x), CTRL, 0xf, 0xf, true));
}
template <int CTRL> __device__ __forceinline__ int dppi(int x) { return __builtin_amdgcn_mov_dpp(x, CTRL, 0xf, 0xf, true); }
constexpr int XOR1 = 0xB1, XOR2 = 0x4E, XOR7 = 0x141, XOR8 = 0x128, ROR4 = 0x124;
__device__ __forceinline__ float tr32(float a, float b) {
  auto t = __builtin_amdgcn_permlane32_swap(__float_as_uint(a), __float_as_uint(b), false, false);
  return __uint_as_float(t[0]) + __uint_as_float(t[1]);
}
__device__ __forceinline__ float tr16(float a, float b) {
  auto t = __builtin_amdgcn_permlane16_swap(__float_as_uint(a), __float_as_uint(b), false, false);
  return __uint_as_float(t[0]) + __uint_as_float(t[1]);
}
__device__ __forceinline__ float xrow16_max(float x) {
  auto s = __builtin_amdgcn_permlane16_swap(__float_as_uint(x), __float_as_uint(x), false, false);
  x = fmaxf(__uint_as_float(s[0]), __uint_as_float(s[1]));
  auto t = __builtin_amdgcn_permlane32_swap(__float_as_uint(x), __float_as_uint(x), false, false);
  return fmaxf(__uint_as_float(t[0]), __uint_as_float(t[1]));
}
__device__ __forceinline__ float lo16(unsigned u) { return __builtin_bit_cast(float, u << 16); }
__device__ __forceinline__ float hi16(unsigned u) { return __builtin_bit_cast(float, u & 0xffff0000u); }
__device__ __forceinline__ rsrc_t mkr(const void* p, int bytes) { return __builtin_amdgcn_make_buffer_rsrc((void*)p, 0, bytes, 0x00020000); }
template <int AUX> __device__ __forceinline__ v4u ldb(rsrc_t r, int voff, int soff) { return __builtin_amdgcn_raw_buffer_load_b128(r, voff, soff, AUX); }
template <int AUX> __device__ __forceinline__ v2u ldb2(rsrc_t r, int voff, int soff) { return __builtin_amdgcn_raw_buffer_load_b64(r, voff, soff, AUX); }
constexpr int NT = 2, SC1 = 16;
__device__ __forceinline__ unsigned gpack(float v, unsigned tb) { return ((__float_as_uint(v) + 0x80u) & 0xffffff00u) | tb; }
__device__ __forceinline__ float gval(unsigned g) { return __uint_as_float(g & 0xffffff00u); }
__device__ __forceinline__ unsigned binc(unsigned e) { return ((e & 0x7f7f7f7fu) + 0x01010101u) ^ (e & 0x80808080u); }
__device__ __forceinline__ unsigned now() { return (unsigned)__builtin_amdgcn_s_memrealtime(); }

#define SB() __builtin_amdgcn_sched_barrier(0)
#define MEMFENCE() asm volatile("" ::: "memory")
#define PIN(x) asm volatile("" : "+v"(x))
#define BAR() do { SB(); asm volatile("s_waitcnt lgkmcnt(0)" ::: "memory"); __builtin_amdgcn_s_barrier(); asm volatile("" ::: "memory"); SB(); } while (0)
template <int N> struct IC { static constexpr int v = N; };
template <int LO, int HI, class F> __device__ __forceinline__ void sfor(F&& f) { if constexpr (LO < HI) { f(IC<LO>{}); sfor<LO + 1, HI>(f); } }
template <int N> __device__ __forceinline__ unsigned pacestep(unsigned t0) {
  unsigned d;
  asm volatile("s_waitcnt vmcnt(%2)\n\ts_memrealtime vcc\n\ts_waitcnt lgkmcnt(0)\n\ts_sub_i32 %0, vcc_lo, %1\n\t"
               "s_cmpk_ge_i32 %0, %6\n\ts_cbranch_scc1 1f\n\ts_waitcnt vmcnt(%3)\n\t"
               "s_cmpk_ge_i32 %0, %7\n\ts_cbranch_scc1 1f\n\ts_waitcnt vmcnt(%4)\n\t"
               "s_cmpk_ge_i32 %0, %8\n\ts_cbranch_scc1 1f\n\ts_waitcnt vmcnt(%5)\n1:"
               : "=s"(d)
               : "s"(t0), "n"(pace(N, 5)), "n"(pace(N, 4)), "n"(pace(N, 3)), "n"(pace(N, 2)), "n"(thr(N, 5)), "n"(thr(N, 4)), "n"(thr(N, 3))
               : "scc", "vcc");
  SB();
  return d;
}

constexpr int OPS = 36;
constexpr int L_QP = 0;
constexpr int L_OP = 512;
constexpr int L_ML = L_OP + 16 * OPS;
constexpr int L_Q = L_ML + 16;
constexpr int L_FLG = L_Q + 64;
constexpr int L_END = L_FLG + 24;
constexpr int POLLW = 7;

__global__ void __launch_bounds__(512, 2) __attribute__((aligned(4096))) k_fused(Params P) {
  __shared__ __attribute__((aligned(16))) float lds[L_END];
  volatile LAS unsigned* flg = (volatile LAS unsigned*)(lds + L_FLG);
  const u64x8 ka = *(const CAS u64x8*)__builtin_amdgcn_kernarg_segment_ptr();
  SB();
  const int tid = threadIdx.x, lane = tid & 63, w = __builtin_amdgcn_readfirstlane(tid >> 6);
  const int r = lane >> 4, c = lane & 15;
  const int bid = blockIdx.x, xcd = bid & 7, jj = bid >> 3, hp = xcd * 8 + (jj >> 2);
  const int br = hp >> 5, h = hp & 31, b = (jj >> 1) & 1, sp = jj & 1;
  const int grp = br * 2 + b, pidx = h * 2 + sp;
  constexpr float L2E = 1.4426950408889634f;
  unsigned kvoff = (((b * NH + h) * SK + (SK - WIN) + sp * 1024 + w * 16) * DH) * 2;
  unsigned wqoff = (64 * w * HD + h * DH + 32 * (jj & 3)) * 2, xoff = 64 * w * 2;
  int lo = lane * 16;
  int qo = 4 * (lane >> 2) * (HD * 2) + (lane & 3) * 16;
  const int oo = 8 * r * (DM * 2) + c * 8;
  if (lane < 3) flg[lane * 8 + w] = 0u;
  asm volatile("" : "+s"(kvoff), "+s"(wqoff), "+s"(xoff));
  asm volatile("" : "+v"(lo), "+v"(qo));
  SB();
  const int kvbytes = 1024 * 256 - w * 4096;
  const rsrc_t wqr = mkr((const void*)((br ? ka[3] : ka[2]) + wqoff), 63 * HD * 2 + 64);
  rsrc_t kr, vr, wor;
#define KVOFF(n) (((n) >> 2) * 32768 + ((n) & 3) * 1024)
#define LDK(n) ldb<NT>(kr, lo, KVOFF(n))
#define LDV(n) ldb<NT>(vr, lo, KVOFF(n))
#define LDQ(n) ldb<NT>(wqr, qo, (n) * (HD * 2))
#define LDO(n) ldb2<0>(wor, oo, (((((n) >> 3) + jj) & 3) << 15) + ((n) & 7) * (DM * 2))

  v4u A[4], K[32], V[32];
  v2u O[32];
  asm volatile("" :: "s"(wqr));
  SB();
  const unsigned long long t0l = __builtin_amdgcn_s_memrealtime();
  SB();
  const u64x4 kb = *((const CAS u64x4*)__builtin_amdgcn_kernarg_segment_ptr() + 2);
  SB();
  sfor<0, 4>([&](auto J) { A[J.v] = LDQ(J.v); SB(); });
  wor = mkr((const void*)((br ? ka[1] : ka[0]) + (h * DH * DM + 64 * w) * 2), 127 * DM * 2 + 128);
  const rsrc_t xr = mkr((const void*)((br ? ka[5] : ka[4]) + xoff), DM * 2 + 128);
  const rsrc_t wsr = mkr((const void*)ka[6], WS_END);
  v2u x0, x1, epv;
  BAR();
  asm volatile("" :: "s"(t0l));
  const unsigned t0 = (unsigned)t0l;
  vr = mkr((const void*)((br ? kb[1] : kb[0]) + kvoff), kvbytes);
  kr = mkr((const void*)((br ? kb[3] : kb[2]) + kvoff), kvbytes);

  v4u q;
  float x[16], s[2], p[2], acc[8], m, lam;
  v2u qg;
  v8u WB[4][2];
  unsigned tg0, tg1;
  LAS unsigned* wrp; const LAS v4u* rdp; int yso, ysb, glo, sidx; unsigned tgb;
  unsigned short* outp = (unsigned short*)ka[7] + b * 1024 + br * 512 + (pidx >> 3) * 64 + (pidx & 1) * 32 + ((pidx >> 1) & 3);
#pragma unroll
  for (int i = 0; i < 8; ++i) acc[i] = 0.f;

  auto QK = [&](auto I) {
    constexpr int n = I.v;
    float d = 0.f;
#pragma unroll
    for (int k = 0; k < 4; ++k) d = dot2(K[n][k], q[k], d);
    x[n & 15] = d;
    PIN(x[n & 15]);
  };
  auto TR = [&](int hf) {
#define TR_STEP(HALF, CTRL, BIT) _Pragma("unroll") for (int hh = 0; hh < (HALF); ++hh) { \
      const float lo_ = x[hh], hi_ = x[hh + (HALF)]; const float t1_ = lo_ + dpp<CTRL>(lo_), t2_ = hi_ + dpp<CTRL>(hi_); \
      x[hh] = (c & (BIT)) ? t2_ : t1_; }
    TR_STEP(8, XOR8, 8) TR_STEP(4, XOR7, 4) TR_STEP(2, XOR2, 2) TR_STEP(1, XOR1, 1)
#undef TR_STEP
    s[hf] = rbf(rbf(x[0]) * 0.08838834764831845f);
    PIN(s[hf]);
  };
  auto PV = [&](auto I) {
    constexpr int j = I.v;
    const float pj = dpp<0x150 + (j & 15)>(p[j >> 4]);
#pragma unroll
    for (int k = 0; k < 4; ++k) { acc[2 * k] = fmaf(pj, lo16(V[j][k]), acc[2 * k]); acc[2 * k + 1] = fmaf(pj, hi16(V[j][k]), acc[2 * k + 1]); }
#pragma unroll
    for (int k = 0; k < 8; ++k) PIN(acc[k]);
    SB();
  };
  auto WP = [&](auto I) {
    constexpr int i = I.v;
    WB[0][i >> 3][i & 7] = __builtin_amdgcn_perm(O[2 * i + 1][0], O[2 * i][0], 0x05040100u);
    WB[1][i >> 3][i & 7] = __builtin_amdgcn_perm(O[2 * i + 1][0], O[2 * i][0], 0x07060302u);
    WB[2][i >> 3][i & 7] = __builtin_amdgcn_perm(O[2 * i + 1][1], O[2 * i][1], 0x05040100u);
    WB[3][i >> 3][i & 7] = __builtin_amdgcn_perm(O[2 * i + 1][1], O[2 * i][1], 0x07060302u);
    if constexpr ((i & 7) == 7) { PIN(WB[0][i >> 3]); PIN(WB[1][i >> 3]); PIN(WB[2][i >> 3]); PIN(WB[3][i >> 3]); }
  };

  sfor<DINIT, NSTEP>([&](auto I) {
    constexpr int n = I.v;
    const unsigned tn = pacestep<n>(t0);
    if constexpr (n >= K0 && n < V0) K[n - K0] = LDK(n - K0);
    if constexpr (n >= V0) V[n - V0] = LDV(n - V0);
    if constexpr (n == QLD) qg = ldb2<SC1>(wsr, w == 1 ? lane * 8 : OOB, WS_Q + hp * 1024 + b * 512);
    SB();
    sfor<wo_lo(n), wo_lo(n + 1)>([&](auto J) { O[J.v] = LDO(J.v); });
    if constexpr (n == XS) {
      SB();
      x0 = ldb2<0>(xr, 8 * (lane >> 2), 0); x1 = ldb2<0>(xr, 8 * (lane >> 2), DM * 2);
      epv = ldb2<SC1>(wsr, 0, WS_EPOCH + 256 * br);
    }
    if constexpr (n == NSTEP - 1)
      __builtin_amdgcn_raw_buffer_store_b128(v4u{__float_as_uint(lam), tg0, tn + t0, tg1}, wsr, tid ? OOB : 0, WS_L + (grp * 64 + pidx) * 16, SC1);
    SB();
    if constexpr (n == QCMP) {
      float qa[16];
#pragma unroll
      for (int k = 0; k < 4; ++k) {
        const unsigned l0 = __builtin_amdgcn_perm(A[1][k], A[0][k], 0x05040100u), h0 = __builtin_amdgcn_perm(A[1][k], A[0][k], 0x07060302u);
        const unsigned l1 = __builtin_amdgcn_perm(A[3][k], A[2][k], 0x05040100u), h1 = __builtin_amdgcn_perm(A[3][k], A[2][k], 0x07060302u);
        qa[2 * k] = dot2(l1, x0[1], dot2(l0, x0[0], 0.f));
        qa[2 * k + 1] = dot2(h1, x0[1], dot2(h0, x0[0], 0.f));
        qa[8 + 2 * k] = dot2(l1, x1[1], dot2(l0, x1[0], 0.f));
        qa[8 + 2 * k + 1] = dot2(h1, x1[1], dot2(h0, x1[0], 0.f));
      }
      float t[8];
#pragma unroll
      for (int i = 0; i < 8; ++i) t[i] = tr32(qa[i], qa[8 + i]);
      f32x4 u;
#pragma unroll
      for (int i = 0; i < 4; ++i) { u[i] = tr16(t[i], t[4 + i]); u[i] += dpp<XOR8>(u[i]); u[i] += dpp<ROR4>(u[i]); }
      if (!(lane & 12)) *(f32x4*)(lds + L_QP + w * 64 + (r >> 1) * 32 + 8 * (lane & 3) + 4 * (r & 1)) = u;
      tg0 = binc(__builtin_amdgcn_readfirstlane(epv[0])); tg1 = binc(__builtin_amdgcn_readfirstlane(epv[1]));
      asm volatile("" : "+s"(outp), "+s"(tg0), "+s"(tg1) :: "memory");
      if (lane == 0) flg[w] = 1u;
    }
    if constexpr (n == QPUB) {
      f32x2 a = {0.f, 0.f};
      if (w == 0) {
        while (!__all(flg[lane & 7] != 0u)) __builtin_amdgcn_s_sleep(1);
        MEMFENCE();
#pragma unroll
        for (int ww = 0; ww < 8; ++ww) a += *(const f32x2*)(lds + L_QP + ww * 64 + 2 * (lane & 31));
      }
      __builtin_amdgcn_raw_buffer_store_b64(v2u{cvtpk(a[0], a[1]), (lane & 1) ? tg1 : tg0}, wsr, tid < 32 ? r * 512 + c * 8 : OOB, WS_Q + hp * 1024 + (jj & 3) * 128, SC1);
    }
    if constexpr (n == QGOT) {
      if (w == 1) {
        const unsigned want = (lane & 1) ? tg1 : tg0;
        if (__builtin_expect(__builtin_amdgcn_ballot_w64(qg[1] != want) != 0ull, 0)) {
          unsigned spins = 0;
#pragma nounroll
          do {
            __builtin_amdgcn_s_sleep(8); MEMFENCE();
            qg = ldb2<SC1>(wsr, lane * 8, WS_Q + hp * 1024 + b * 512);
          } while (__builtin_amdgcn_ballot_w64(qg[1] != want) != 0ull && ++spins < SPIN_CAP);
        }
        ((unsigned*)lds)[L_Q + lane] = qg[0];
        MEMFENCE();
        if (lane == 0) flg[16] = 1u;
      }
    }
    if constexpr (n == QRD) {
      while (flg[16] == 0u) __builtin_amdgcn_s_sleep(1);
      MEMFENCE();
      q = *(const v4u*)(lds + L_Q + 4 * c);
      PIN(q);
    }
    if constexpr (n > QRD && n <= QRD + 2) sfor<8 * (n - QRD - 1), 8 * (n - QRD - 1) + 8>(QK);
    if constexpr (n == QRD + 3) TR(0);
    if constexpr (n >= SMAX - 3 && n <= SMAX) sfor<16 + 4 * (n - (SMAX - 3)), 20 + 4 * (n - (SMAX - 3))>(QK);
    if constexpr (n == SMAX) {
      TR(1);
      m = fmaxf(s[0], s[1]);
      m = fmaxf(m, dpp<XOR1>(m)); m = fmaxf(m, dpp<XOR2>(m)); m = fmaxf(m, dpp<XOR7>(m)); m = fmaxf(m, dpp<XOR8>(m));
      m = xrow16_max(m);
      p[0] = __builtin_amdgcn_exp2f((s[0] - m) * L2E);
      p[1] = __builtin_amdgcn_exp2f((s[1] - m) * L2E);
      float l = p[0] + p[1];
      l += dpp<XOR1>(l); l += dpp<XOR2>(l); l += dpp<XOR7>(l); l += dpp<XOR8>(l);
      l = tr16(l, l); l = tr32(l, l);
      if (lane == 0) { *(f32x2*)(lds + L_ML + 2 * w) = f32x2{m, l}; MEMFENCE(); flg[8 + w] = 1u; }
    }
    if constexpr (n == FRD) {
      while (!__all(flg[8 + (lane & 7)] != 0u)) __builtin_amdgcn_s_sleep(1);
      MEMFENCE();
      const f32x2 ml = *(const f32x2*)(lds + L_ML + 2 * (lane & 7));
      float M = ml[0];
      M = fmaxf(M, dpp<XOR1>(M)); M = fmaxf(M, dpp<XOR2>(M)); M = fmaxf(M, dpp<XOR7>(M));
      float L = ml[1] * __builtin_amdgcn_exp2f((ml[0] - M) * L2E);
      L += dpp<XOR1>(L); L += dpp<XOR2>(L); L += dpp<XOR7>(L);
      const float f = __builtin_amdgcn_exp2f((m - M) * L2E) / L;
      p[0] *= f; p[1] *= f;
#pragma unroll
      for (int k = 0; k < 8; ++k) { acc[k] *= f; PIN(acc[k]); }
      lam = fmaf(M, L2E, __builtin_amdgcn_logf(L));
      PIN(lam); PIN(p[0]); PIN(p[1]);
    }
    sfor<wo_pairs(n - 1), wo_pairs(n)>(WP);
    if constexpr (n == NSTEP - 2) {
      wrp = (LAS unsigned*)lds + L_OP + (w + 8 * (r & 1)) * OPS + ((2 * c + (r >> 1) - 8 * jj) & 31);
      rdp = (const LAS v4u*)((LAS unsigned*)lds + L_OP + c * OPS + 4 * r);
      sidx = (c & 8) ? 0xEEEE : 0x4444;
      yso = lane * 4;
      ysb = WS_Y + (grp * 64 + pidx) * 2048 + 256 * w;
      tgb = (((c & 4) ? tg1 : tg0) >> (8 * (c & 3))) & 0xffu;
      glo = w == POLLW ? lo : OOB;
      asm volatile("" : "+v"(wrp), "+v"(rdp), "+v"(yso), "+v"(tgb), "+v"(glo), "+v"(sidx));
      asm volatile("" : "+s"(ysb));
    }
    SB();
    if constexpr (n > SMAX) sfor<pvdone(n - 1), pvdone(n)>(PV);
    SB();
  });
  f32x4 d[4];
#pragma unroll
  for (int t = 0; t < 4; ++t) { d[t] = f32x4{0.f, 0.f, 0.f, 0.f}; PIN(d[t]); }
  sfor<pvdone(NSTEP - 1), 32>(PV);
  {
    float t[4];
#pragma unroll
    for (int i = 0; i < 4; ++i) t[i] = tr32(acc[i], acc[4 + i]);
    *wrp = cvtpk(tr16(t[0], t[2]), tr16(t[1], t[3]));
  }
  SB();
  v4u gl = ldb<SC1>(wsr, glo, WS_L + grp * 1024);
  BAR();
  {
    const v4u af0 = rdp[0], af1 = rdp[4];
    SB();
#pragma unroll
    for (int t = 0; t < 4; ++t) d[t] = __builtin_amdgcn_smfmac_f32_16x16x64_bf16(__builtin_bit_cast(bf16x8, af0), __builtin_bit_cast(bf16x16, WB[t][0]), d[t], sidx, 0, 0);
    SB();
#pragma unroll
    for (int t = 0; t < 4; ++t) d[t] = __builtin_amdgcn_smfmac_f32_16x16x64_bf16(__builtin_bit_cast(bf16x8, af1), __builtin_bit_cast(bf16x16, WB[t][1]), d[t], sidx, 0, 0);
    SB();
    float e[4];
#pragma unroll
    for (int t = 0; t < 4; ++t) { const f32x2 h2 = f32x2{d[t][0], d[t][1]} + f32x2{d[t][2], d[t][3]}; e[t] = h2[0] + h2[1]; }
    const float y = tr16(tr32(e[0], e[2]), tr32(e[1], e[3]));
    __builtin_amdgcn_raw_buffer_store_b32(gpack(y, tgb), wsr, yso, ysb, SC1);
  }
  if (w == POLLW) {
    const unsigned ts = now();
    unsigned spins = 0;
    if (__builtin_amdgcn_ballot_w64(((gl[1] ^ tg0) | (gl[3] ^ tg1)) != 0u) != 0ull) {
#pragma nounroll
      do {
        MEMFENCE();
        gl = ldb<SC1>(wsr, lo, WS_L + grp * 1024);
      } while (__builtin_amdgcn_ballot_w64(((gl[1] ^ tg0) | (gl[3] ^ tg1)) != 0u) != 0ull && ++spins < SPIN_CAP);
    }
    const float la = __uint_as_float(gl[0]);
    const float wl = __builtin_amdgcn_rcpf(1.0f + __builtin_amdgcn_exp2f(dpp<XOR1>(la) - la));
    float wt[2];
#pragma unroll
    for (int i = 0; i < 2; ++i) wt[i] = __int_as_float(__builtin_amdgcn_ds_bpermute(((lane >> 1) + 32 * i) * 4, __float_as_int(wl)));
    int dl = (int)(gl[2] - ts), dm = dl;
    dl = max(dl, dppi<XOR1>(dl)); dl = max(dl, dppi<XOR2>(dl)); dl = max(dl, dppi<XOR7>(dl)); dl = max(dl, dppi<XOR8>(dl));
    dl = max(max(__builtin_amdgcn_readlane(dl, 0), __builtin_amdgcn_readlane(dl, 16)), max(__builtin_amdgcn_readlane(dl, 32), __builtin_amdgcn_readlane(dl, 48)));
    dm += dppi<XOR1>(dm); dm += dppi<XOR2>(dm); dm += dppi<XOR7>(dm); dm += dppi<XOR8>(dm);
    dm = ((__builtin_amdgcn_readlane(dm, 0) + __builtin_amdgcn_readlane(dm, 16)) + (__builtin_amdgcn_readlane(dm, 32) + __builtin_amdgcn_readlane(dm, 48))) >> 6;
    dl = min(max(max(dl + PLEAD, dm + PLMEAN), PMIN), 400);
    int yo = (lane >> 1) * 2048 + (lane & 1) * 16, yb = WS_Y + grp * 64 * 2048 + pidx * 32, yb1 = yb + 32 * 2048;
    unsigned want = (lane & 1) ? tg1 : tg0;
    asm volatile("" : "+s"(yb), "+s"(yb1));
    const rsrc_t outr = mkr(outp, 58);
    int oso = (lane & 14) ? OOB : (lane & 1) * 32 + (lane >> 4) * 8;
    asm volatile("" : "+v"(yo), "+v"(want), "+v"(oso), "+v"(wt[0]), "+v"(wt[1]));
    while ((int)(now() - ts) < dl) {}
#define YBAD ((((__builtin_amdgcn_perm(gy0[1], gy0[0], 0x0c0c0400u) | __builtin_amdgcn_perm(gy0[3], gy0[2], 0x04000c0cu)) ^ want) | \
              ((__builtin_amdgcn_perm(gy1[1], gy1[0], 0x0c0c0400u) | __builtin_amdgcn_perm(gy1[3], gy1[2], 0x04000c0cu)) ^ want)) != 0u)
    MEMFENCE();
    v4u gy0 = ldb<SC1>(wsr, yo, yb), gy1 = ldb<SC1>(wsr, yo, yb1);
    if (__builtin_expect(__builtin_amdgcn_ballot_w64(YBAD) != 0ull, 0)) {
#pragma nounroll
      do {
        MEMFENCE();
        gy0 = ldb<SC1>(wsr, yo, yb); gy1 = ldb<SC1>(wsr, yo, yb1);
      } while (__builtin_amdgcn_ballot_w64(YBAD) != 0ull && ++spins < SPIN_CAP);
    }
#undef YBAD
    const f32x2 a01 = __builtin_elementwise_fma(f32x2{wt[1], wt[1]}, f32x2{gval(gy1[0]), gval(gy1[1])}, wt[0] * f32x2{gval(gy0[0]), gval(gy0[1])});
    const f32x2 a23 = __builtin_elementwise_fma(f32x2{wt[1], wt[1]}, f32x2{gval(gy1[2]), gval(gy1[3])}, wt[0] * f32x2{gval(gy0[2]), gval(gy0[3])});
    const float a[4] = {a01[0], a01[1], a23[0], a23[1]};
    float z = tr16(tr32(a[0], a[2]), tr32(a[1], a[3]));
    z += dpp<XOR2>(z); z += dpp<ROR4>(z); z += dpp<XOR8>(z);
    __builtin_amdgcn_raw_buffer_store_b16((unsigned short)cvtpk(z, z), outr, oso, 0, 0);
    if (lane == 0 && pidx == 0 && b == 0) __builtin_amdgcn_raw_buffer_store_b64(v2u{tg0, tg1}, wsr, 0, WS_EPOCH + 256 * br, SC1);
  }
}

extern "C" void kernel_launch(void* const* d_in, const int* in_sizes, int n_in,
                              void* d_out, int out_size, void* d_ws, size_t ws_size,
                              hipStream_t stream) {
  Params P;
  for (int i = 0; i < 2; ++i) {
    P.x[i] = (const unsigned short*)d_in[5 * i + 0];
    P.k[i] = (const unsigned short*)d_in[5 * i + 1];
    P.v[i] = (const unsigned short*)d_in[5 * i + 2];
    P.wq[i] = (const unsigned short*)d_in[5 * i + 3];
    P.wo[i] = (const unsigned short*)d_in[5 * i + 4];
  }
  P.out = (unsigned short*)d_out;
  P.ws = (unsigned char*)d_ws;
  hipLaunchKernelGGL(k_fused, dim3(256), dim3(512), 0, stream, P);
}
```

```cpp
#include <hip/hip_runtime.h>
#include <hip/hip_bf16.h>
#include <stdint.h>

typedef unsigned v4u __attribute__((ext_vector_type(4)));
typedef unsigned v2u __attribute__((ext_vector_type(2)));
typedef unsigned v8u __attribute__((ext_vector_type(8)));
typedef float f32x4 __attribute__((ext_vector_type(4)));
typedef float f32x2 __attribute__((ext_vector_type(2)));
typedef __bf16 bf16x2 __attribute__((ext_vector_type(2)));
typedef __bf16 bf16x8 __attribute__((ext_vector_type(8)));
typedef __bf16 bf16x16 __attribute__((ext_vector_type(16)));
typedef unsigned long long u64x8 __attribute__((ext_vector_type(8)));
typedef unsigned long long u64x4 __attribute__((ext_vector_type(4)));
typedef __amdgpu_buffer_rsrc_t rsrc_t;
#define LAS __attribute__((address_space(3)))
#define CAS __attribute__((address_space(4)))

constexpr int SK = 8192, WIN = 2048, DH = 128, NH = 32, DM = 512, HD = 4096;

constexpr int K0 = 8, V0 = 40, NSTEP = 72;
constexpr int DINIT = 4;
constexpr int DMAX = 5;
constexpr int RATE = 240, OFFS = -550;
constexpr int RATEB = 280, OFFSB = -50;
constexpr int XR4 = 22, GAP = 120;
constexpr int XS = 9;
constexpr int QPUB = 15;
constexpr int QLD = 23;
constexpr int QRD = 31;
constexpr int WO0 = 11, WON = 2, WOD = 3;
constexpr int FRD = 58;
constexpr int PMIN = 12;
constexpr int PLEAD = 118;
constexpr int PLMEAN = 178;
constexpr int SMAX = K0 + 31 + DMAX;
constexpr int clampi(int v, int lo, int hi) { return v < lo ? lo : v > hi ? hi : v; }
constexpr int wo_lo(int n) { return clampi((n - 4) * 2, 0, 8) + clampi(((n - WO0) * WON + WOD - 1) / WOD, 0, 24); }
constexpr int nmain(int n) { return (n >= 4 && n < K0) ? 2 : 1; }
constexpr int nops(int n) { return ((n < 4 || n >= K0) ? 1 : 0) + wo_lo(n + 1) - wo_lo(n) + (n == XS ? 3 : 0) + (n == QPUB) + (n == QLD); }
constexpr int pace(int n, int d) { if (d > n) return 63; int c = -nmain(n - d); for (int i = n - d; i < n; ++i) c += nops(i); return c; }
constexpr int dith(int n) { constexpr int t[4] = {0, 2, 1, 3}; return t[n & 3]; }
constexpr int thr(int n, int d) {
  const int a = OFFS + n * RATE, b = OFFSB + n * RATEB, e = n * XR4;
  return ((d == 3 ? a + ((b - a) * dith(n)) / 3 : d == 4 ? b + (e * dith(n)) / 3 : b + e) + (d - 3) * GAP) / 10;
}
constexpr int wo_pairs(int n) { return wo_lo(n - DMAX) / 2; }
constexpr int QCMP = XS + 4, QGOT = QLD + DMAX + 1;
constexpr int pvdone(int n) {
  int d = 0;
  for (int i = SMAX + 1; i <= n; ++i) d = clampi(i - DMAX - V0 + 1, 0, d + 2);
  return d;
}
static_assert(QCMP < QPUB && QPUB < QLD && QGOT < QRD && QRD + 3 <= SMAX - 3 && wo_pairs(NSTEP - 2) == 16 && SMAX < FRD && FRD < NSTEP - 2 && WO0 >= K0 && DINIT == 4 && XS >= DINIT, "schedule");

constexpr int WS_EPOCH = 0;
constexpr int WS_L = 4096;
constexpr int WS_Y = 8192;
constexpr int WS_Q = WS_Y + 256 * 2048;
constexpr int WS_END = WS_Q + 64 * 1024;
constexpr unsigned SPIN_CAP = 1u << 18;
constexpr int OOB = 0x7fffff00;

struct Params {
  const unsigned short* wo[2];
  const unsigned short* wq[2];
  const unsigned short* x[2];
  unsigned char* ws;
  unsigned short* out;
  const unsigned short* v[2];
  const unsigned short* k[2];
};

__device__ __forceinline__ float dot2(unsigned a, unsigned b, float acc) {
  return __builtin_amdgcn_fdot2_f32_bf16(__builtin_bit_cast(bf16x2, a), __builtin_bit_cast(bf16x2, b), acc, false);
}
__device__ __forceinline__ unsigned cvtpk(float lo, float hi) {
  bf16x2 r; r.x = (__bf16)lo; r.y = (__bf16)hi; return __builtin_bit_cast(unsigned, r);
}
__device__ __forceinline__ float rbf(float x) { return (float)(__bf16)x; }
template <int CTRL> __device__ __forceinline__ float dpp(float x) {
  return __builtin_bit_cast(float, __builtin_amdgcn_mov_dpp(__builtin_bit_cast(int, x), CTRL, 0xf, 0xf, true));
}
template <int CTRL> __device__ __forceinline__ int dppi(int x) { return __builtin_amdgcn_mov_dpp(x, CTRL, 0xf, 0xf, true); }
constexpr int XOR1 = 0xB1, XOR2 = 0x4E, XOR7 = 0x141, XOR8 = 0x128, ROR4 = 0x124;
__device__ __forceinline__ float tr32(float a, float b) {
  auto t = __builtin_amdgcn_permlane32_swap(__float_as_uint(a), __float_as_uint(b), false, false);
  return __uint_as_float(t[0]) + __uint_as_float(t[1]);
}
__device__ __forceinline__ float tr16(float a, float b) {
  auto t = __builtin_amdgcn_permlane16_swap(__float_as_uint(a), __float_as_uint(b), false, false);
  return __uint_as_float(t[0]) + __uint_as_float(t[1]);
}
__device__ __forceinline__ float xrow16_max(float x) {
  auto s = __builtin_amdgcn_permlane16_swap(__float_as_uint(x), __float_as_uint(x), false, false);
  x = fmaxf(__uint_as_float(s[0]), __uint_as_float(s[1]));
  auto t = __builtin_amdgcn_permlane32_swap(__float_as_uint(x), __float_as_uint(x), false, false);
  return fmaxf(__uint_as_float(t[0]), __uint_as_float(t[1]));
}
__device__ __forceinline__ float lo16(unsigned u) { return __builtin_bit_cast(float, u << 16); }
__device__ __forceinline__ float hi16(unsigned u) { return __builtin_bit_cast(float, u & 0xffff0000u); }
__device__ __forceinline__ rsrc_t mkr(const void* p, int bytes) { return __builtin_amdgcn_make_buffer_rsrc((void*)p, 0, bytes, 0x00020000); }
template <int AUX> __device__ __forceinline__ v4u ldb(rsrc_t r, int voff, int soff) { return __builtin_amdgcn_raw_buffer_load_b128(r, voff, soff, AUX); }
template <int AUX> __device__ __forceinline__ v2u ldb2(rsrc_t r, int voff, int soff) { return __builtin_amdgcn_raw_buffer_load_b64(r, voff, soff, AUX); }
constexpr int NT = 2, SC1 = 16;
__device__ __forceinline__ unsigned gpack(float v, unsigned tb) { return ((__float_as_uint(v) + 0x80u) & 0xffffff00u) | tb; }
__device__ __forceinline__ float gval(unsigned g) { return __uint_as_float(g & 0xffffff00u); }
__device__ __forceinline__ unsigned binc(unsigned e) { return ((e & 0x7f7f7f7fu) + 0x01010101u) ^ (e & 0x80808080u); }
__device__ __forceinline__ unsigned now() { return (unsigned)__builtin_amdgcn_s_memrealtime(); }

#define SB() __builtin_amdgcn_sched_barrier(0)
#define MEMFENCE() asm volatile("" ::: "memory")
#define PIN(x) asm volatile("" : "+v"(x))
#define BAR() do { SB(); asm volatile("s_waitcnt lgkmcnt(0)" ::: "memory"); __builtin_amdgcn_s_barrier(); asm volatile("" ::: "memory"); SB(); } while (0)
template <int N> struct IC { static constexpr int v = N; };
template <int LO, int HI, class F> __device__ __forceinline__ void sfor(F&& f) { if constexpr (LO < HI) { f(IC<LO>{}); sfor<LO + 1, HI>(f); } }
template <int N> __device__ __forceinline__ unsigned pacestep(unsigned t0) {
  unsigned d;
  asm volatile("s_waitcnt vmcnt(%2)\n\ts_memrealtime vcc\n\ts_waitcnt lgkmcnt(0)\n\ts_sub_i32 %0, vcc_lo, %1\n\t"
               "s_cmpk_ge_i32 %0, %6\n\ts_cbranch_scc1 1f\n\ts_waitcnt vmcnt(%3)\n\t"
               "s_cmpk_ge_i32 %0, %7\n\ts_cbranch_scc1 1f\n\ts_waitcnt vmcnt(%4)\n\t"
               "s_cmpk_ge_i32 %0, %8\n\ts_cbranch_scc1 1f\n\ts_waitcnt vmcnt(%5)\n1:"
               : "=s"(d)
               : "s"(t0), "n"(pace(N, 5)), "n"(pace(N, 4)), "n"(pace(N, 3)), "n"(pace(N, 2)), "n"(thr(N, 5)), "n"(thr(N, 4)), "n"(thr(N, 3))
               : "scc", "vcc");
  SB();
  return d;
}

constexpr int OPS = 36;
constexpr int L_QP = 0;
constexpr int L_OP = 512;
constexpr int L_ML = L_OP + 16 * OPS;
constexpr int L_Q = L_ML + 16;
constexpr int L_FLG = L_Q + 64;
constexpr int L_END = L_FLG + 24;
constexpr int POLLW = 7;

__global__ void __launch_bounds__(512, 2) __attribute__((aligned(4096))) k_fused(Params P) {
  __shared__ __attribute__((aligned(16))) float lds[L_END];
  volatile LAS unsigned* flg = (volatile LAS unsigned*)(lds + L_FLG);
  const u64x8 ka = *(const CAS u64x8*)__builtin_amdgcn_kernarg_segment_ptr();
  SB();
  const int tid = threadIdx.x, lane = tid & 63, w = __builtin_amdgcn_readfirstlane(tid >> 6);
  const int r = lane >> 4, c = lane & 15;
  const int bid = blockIdx.x, xcd = bid & 7, jj = bid >> 3, hp = xcd * 8 + (jj >> 2);
  const int br = hp >> 5, h = hp & 31, b = (jj >> 1) & 1, sp = jj & 1;
  const int grp = br * 2 + b, pidx = h * 2 + sp;
  constexpr float L2E = 1.4426950408889634f;
  unsigned kvoff = (((b * NH + h) * SK + (SK - WIN) + sp * 128 + w * 16) * DH) * 2;
  unsigned wqoff = (64 * w * HD + h * DH + 32 * (jj & 3)) * 2, xoff = 64 * w * 2;
  int lo = lane * 16;
  int qo = 4 * (lane >> 2) * (HD * 2) + (lane & 3) * 16;
  const int oo = 8 * r * (DM * 2) + c * 8;
  if (lane < 3) flg[lane * 8 + w] = 0u;
  asm volatile("" : "+s"(kvoff), "+s"(wqoff), "+s"(xoff));
  asm volatile("" : "+v"(lo), "+v"(qo));
  SB();
  const int kvbytes = (2048 - 128) * 256 - w * 4096;
  const int rsub = ((h >> 1) + sp) << 10;
  const rsrc_t wqr = mkr((const void*)((br ? ka[3] : ka[2]) + wqoff), 63 * HD * 2 + 64);
  rsrc_t kr, vr, wor;
#define KVOFF(n) (((n) >> 2) * 65536 + (((((n) & 3) << 10) + rsub) & 0xc00))
#define LDK(n) ldb<NT>(kr, lo, KVOFF(n))
#define LDV(n) ldb<NT>(vr, lo, KVOFF(n))
#define LDQ(n) ldb<NT>(wqr, qo, (n) * (HD * 2))
#define LDO(n) ldb2<0>(wor, oo, (((((n) >> 3) + jj) & 3) << 15) + ((n) & 7) * (DM * 2))

  v4u A[4], K[32], V[32];
  v2u O[32];
  asm volatile("" :: "s"(wqr));
  SB();
  const unsigned long long t0l = __builtin_amdgcn_s_memrealtime();
  SB();
  const u64x4 kb = *((const CAS u64x4*)__builtin_amdgcn_kernarg_segment_ptr() + 2);
  SB();
  sfor<0, 4>([&](auto J) { A[J.v] = LDQ(J.v); SB(); });
  wor = mkr((const void*)((br ? ka[1] : ka[0]) + (h * DH * DM + 64 * w) * 2), 127 * DM * 2 + 128);
  const rsrc_t xr = mkr((const void*)((br ? ka[5] : ka[4]) + xoff), DM * 2 + 128);
  const rsrc_t wsr = mkr((const void*)ka[6], WS_END);
  v2u x0, x1, epv;
  BAR();
  asm volatile("" :: "s"(t0l));
  const unsigned t0 = (unsigned)t0l;
  vr = mkr((const void*)((br ? kb[1] : kb[0]) + kvoff), kvbytes);
  kr = mkr((const void*)((br ? kb[3] : kb[2]) + kvoff), kvbytes);

  v4u q;
  float x[16], s[2], p[2], acc[8], m, lam;
  v2u qg;
  v8u WB[4][2];
  unsigned tg0, tg1;
  LAS unsigned* wrp; const LAS v4u* rdp; int yso, ysb, glo, sidx; unsigned tgb;
  unsigned short* outp = (unsigned short*)ka[7] + b * 1024 + br * 512 + (pidx >> 3) * 64 + (pidx & 1) * 32 + ((pidx >> 1) & 3);
#pragma unroll
  for (int i = 0; i < 8; ++i) acc[i] = 0.f;

  auto QK = [&](auto I) {
    constexpr int n = I.v;
    float d = 0.f;
#pragma unroll
    for (int k = 0; k < 4; ++k) d = dot2(K[n][k], q[k], d);
    x[n & 15] = d;
    PIN(x[n & 15]);
  };
  auto TR = [&](int hf) {
#define TR_STEP(HALF, CTRL, BIT) _Pragma("unroll") for (int hh = 0; hh < (HALF); ++hh) { \
      const float lo_ = x[hh], hi_ = x[hh + (HALF)]; const float t1_ = lo_ + dpp<CTRL>(lo_), t2_ = hi_ + dpp<CTRL>(hi_); \
      x[hh] = (c & (BIT)) ? t2_ : t1_; }
    TR_STEP(8, XOR8, 8) TR_STEP(4, XOR7, 4) TR_STEP(2, XOR2, 2) TR_STEP(1, XOR1, 1)
#undef TR_STEP
    s[hf] = rbf(rbf(x[0]) * 0.08838834764831845f);
    PIN(s[hf]);
  };
  auto PV = [&](auto I) {
    constexpr int j = I.v;
    const float pj = dpp<0x150 + (j & 15)>(p[j >> 4]);
#pragma unroll
    for (int k = 0; k < 4; ++k) { acc[2 * k] = fmaf(pj, lo16(V[j][k]), acc[2 * k]); acc[2 * k + 1] = fmaf(pj, hi16(V[j][k]), acc[2 * k + 1]); }
#pragma unroll
    for (int k = 0; k < 8; ++k) PIN(acc[k]);
    SB();
  };
  auto WP = [&](auto I) {
    constexpr int i = I.v;
    WB[0][i >> 3][i & 7] = __builtin_amdgcn_perm(O[2 * i + 1][0], O[2 * i][0], 0x05040100u);
    WB[1][i >> 3][i & 7] = __builtin_amdgcn_perm(O[2 * i + 1][0], O[2 * i][0], 0x07060302u);
    WB[2][i >> 3][i & 7] = __builtin_amdgcn_perm(O[2 * i + 1][1], O[2 * i][1], 0x05040100u);
    WB[3][i >> 3][i & 7] = __builtin_amdgcn_perm(O[2 * i + 1][1], O[2 * i][1], 0x07060302u);
    if constexpr ((i & 7) == 7) { PIN(WB[0][i >> 3]); PIN(WB[1][i >> 3]); PIN(WB[2][i >> 3]); PIN(WB[3][i >> 3]); }
  };

  sfor<DINIT, NSTEP>([&](auto I) {
    constexpr int n = I.v;
    const unsigned tn = pacestep<n>(t0);
    if constexpr (n >= K0 && n < V0) K[n - K0] = LDK(n - K0);
    if constexpr (n >= V0) V[n - V0] = LDV(n - V0);
    if constexpr (n == QLD) qg = ldb2<SC1>(wsr, w == 1 ? lane * 8 : OOB, WS_Q + hp * 1024 + b * 512);
    SB();
    sfor<wo_lo(n), wo_lo(n + 1)>([&](auto J) { O[J.v] = LDO(J.v); });
    if constexpr (n == XS) {
      SB();
      x0 = ldb2<0>(xr, 8 * (lane >> 2), 0); x1 = ldb2<0>(xr, 8 * (lane >> 2), DM * 2);
      epv = ldb2<SC1>(wsr, 0, WS_EPOCH + 256 * br);
    }
    if constexpr (n == NSTEP - 1)
      __builtin_amdgcn_raw_buffer_store_b128(v4u{__float_as_uint(lam), tg0, tn + t0, tg1}, wsr, tid ? OOB : 0, WS_L + (grp * 64 + pidx) * 16, SC1);
    SB();
    if constexpr (n == QCMP) {
      float qa[16];
#pragma unroll
      for (int k = 0; k < 4; ++k) {
        const unsigned l0 = __builtin_amdgcn_perm(A[1][k], A[0][k], 0x05040100u), h0 = __builtin_amdgcn_perm(A[1][k], A[0][k], 0x07060302u);
        const unsigned l1 = __builtin_amdgcn_perm(A[3][k], A[2][k], 0x05040100u), h1 = __builtin_amdgcn_perm(A[3][k], A[2][k], 0x07060302u);
        qa[2 * k] = dot2(l1, x0[1], dot2(l0, x0[0], 0.f));
        qa[2 * k + 1] = dot2(h1, x0[1], dot2(h0, x0[0], 0.f));
        qa[8 + 2 * k] = dot2(l1, x1[1], dot2(l0, x1[0], 0.f));
        qa[8 + 2 * k + 1] = dot2(h1, x1[1], dot2(h0, x1[0], 0.f));
      }
      float t[8];
#pragma unroll
      for (int i = 0; i < 8; ++i) t[i] = tr32(qa[i], qa[8 + i]);
      f32x4 u;
#pragma unroll
      for (int i = 0; i < 4; ++i) { u[i] = tr16(t[i], t[4 + i]); u[i] += dpp<XOR8>(u[i]); u[i] += dpp<ROR4>(u[i]); }
      if (!(lane & 12)) *(f32x4*)(lds + L_QP + w * 64 + (r >> 1) * 32 + 8 * (lane & 3) + 4 * (r & 1)) = u;
      tg0 = binc(__builtin_amdgcn_readfirstlane(epv[0])); tg1 = binc(__builtin_amdgcn_readfirstlane(epv[1]));
      asm volatile("" : "+s"(outp), "+s"(tg0), "+s"(tg1) :: "memory");
      if (lane == 0) flg[w] = 1u;
    }
    if constexpr (n == QPUB) {
      f32x2 a = {0.f, 0.f};
      if (w == 0) {
        while (!__all(flg[lane & 7] != 0u)) __builtin_amdgcn_s_sleep(1);
        MEMFENCE();
#pragma unroll
        for (int ww = 0; ww < 8; ++ww) a += *(const f32x2*)(lds + L_QP + ww * 64 + 2 * (lane & 31));
      }
      __builtin_amdgcn_raw_buffer_store_b64(v2u{cvtpk(a[0], a[1]), (lane & 1) ? tg1 : tg0}, wsr, tid < 32 ? r * 512 + c * 8 : OOB, WS_Q + hp * 1024 + (jj & 3) * 128, SC1);
    }
    if constexpr (n == QGOT) {
      if (w == 1) {
        const unsigned want = (lane & 1) ? tg1 : tg0;
        if (__builtin_expect(__builtin_amdgcn_ballot_w64(qg[1] != want) != 0ull, 0)) {
          unsigned spins = 0;
#pragma nounroll
          do {
            __builtin_amdgcn_s_sleep(8); MEMFENCE();
            qg = ldb2<SC1>(wsr, lane * 8, WS_Q + hp * 1024 + b * 512);
          } while (__builtin_amdgcn_ballot_w64(qg[1] != want) != 0ull && ++spins < SPIN_CAP);
        }
        ((unsigned*)lds)[L_Q + lane] = qg[0];
        MEMFENCE();
        if (lane == 0) flg[16] = 1u;
      }
    }
    if constexpr (n == QRD) {
      while (flg[16] == 0u) __builtin_amdgcn_s_sleep(1);
      MEMFENCE();
      q = *(const v4u*)(lds + L_Q + 4 * c);
      PIN(q);
    }
    if constexpr (n > QRD && n <= QRD + 2) sfor<8 * (n - QRD - 1), 8 * (n - QRD - 1) + 8>(QK);
    if constexpr (n == QRD + 3) TR(0);
    if constexpr (n >= SMAX - 3 && n <= SMAX) sfor<16 + 4 * (n - (SMAX - 3)), 20 + 4 * (n - (SMAX - 3))>(QK);
    if constexpr (n == SMAX) {
      TR(1);
      m = fmaxf(s[0], s[1]);
      m = fmaxf(m, dpp<XOR1>(m)); m = fmaxf(m, dpp<XOR2>(m)); m = fmaxf(m, dpp<XOR7>(m)); m = fmaxf(m, dpp<XOR8>(m));
      m = xrow16_max(m);
      p[0] = __builtin_amdgcn_exp2f((s[0] - m) * L2E);
      p[1] = __builtin_amdgcn_exp2f((s[1] - m) * L2E);
      float l = p[0] + p[1];
      l += dpp<XOR1>(l); l += dpp<XOR2>(l); l += dpp<XOR7>(l); l += dpp<XOR8>(l);
      l = tr16(l, l); l = tr32(l, l);
      if (lane == 0) { *(f32x2*)(lds + L_ML + 2 * w) = f32x2{m, l}; MEMFENCE(); flg[8 + w] = 1u; }
    }
    if constexpr (n == FRD) {
      while (!__all(flg[8 + (lane & 7)] != 0u)) __builtin_amdgcn_s_sleep(1);
      MEMFENCE();
      const f32x2 ml = *(const f32x2*)(lds + L_ML + 2 * (lane & 7));
      float M = ml[0];
      M = fmaxf(M, dpp<XOR1>(M)); M = fmaxf(M, dpp<XOR2>(M)); M = fmaxf(M, dpp<XOR7>(M));
      float L = ml[1] * __builtin_amdgcn_exp2f((ml[0] - M) * L2E);
      L += dpp<XOR1>(L); L += dpp<XOR2>(L); L += dpp<XOR7>(L);
      const float f = __builtin_amdgcn_exp2f((m - M) * L2E) / L;
      p[0] *= f; p[1] *= f;
#pragma unroll
      for (int k = 0; k < 8; ++k) { acc[k] *= f; PIN(acc[k]); }
      lam = fmaf(M, L2E, __builtin_amdgcn_logf(L));
      PIN(lam); PIN(p[0]); PIN(p[1]);
    }
    sfor<wo_pairs(n - 1), wo_pairs(n)>(WP);
    if constexpr (n == NSTEP - 2) {
      wrp = (LAS unsigned*)lds + L_OP + (w + 8 * (r & 1)) * OPS + ((2 * c + (r >> 1) - 8 * jj) & 31);
      rdp = (const LAS v4u*)((LAS unsigned*)lds + L_OP + c * OPS + 4 * r);
      sidx = (c & 8) ? 0xEEEE : 0x4444;
      yso = lane * 4;
      ysb = WS_Y + (grp * 64 + pidx) * 2048 + 256 * w;
      tgb = (((c & 4) ? tg1 : tg0) >> (8 * (c & 3))) & 0xffu;
      glo = w == POLLW ? lo : OOB;
      asm volatile("" : "+v"(wrp), "+v"(rdp), "+v"(yso), "+v"(tgb), "+v"(glo), "+v"(sidx));
      asm volatile("" : "+s"(ysb));
    }
    SB();
    if constexpr (n > SMAX) sfor<pvdone(n - 1), pvdone(n)>(PV);
    SB();
  });
  f32x4 d[4];
#pragma unroll
  for (int t = 0; t < 4; ++t) { d[t] = f32x4{0.f, 0.f, 0.f, 0.f}; PIN(d[t]); }
  sfor<pvdone(NSTEP - 1), 32>(PV);
  {
    float t[4];
#pragma unroll
    for (int i = 0; i < 4; ++i) t[i] = tr32(acc[i], acc[4 + i]);
    *wrp = cvtpk(tr16(t[0], t[2]), tr16(t[1], t[3]));
  }
  SB();
  v4u gl = ldb<SC1>(wsr, glo, WS_L + grp * 1024);
  BAR();
  {
    const v4u af0 = rdp[0], af1 = rdp[4];
    SB();
#pragma unroll
    for (int t = 0; t < 4; ++t) d[t] = __builtin_amdgcn_smfmac_f32_16x16x64_bf16(__builtin_bit_cast(bf16x8, af0), __builtin_bit_cast(bf16x16, WB[t][0]), d[t], sidx, 0, 0);
    SB();
#pragma unroll
    for (int t = 0; t < 4; ++t) d[t] = __builtin_amdgcn_smfmac_f32_16x16x64_bf16(__builtin_bit_cast(bf16x8, af1), __builtin_bit_cast(bf16x16, WB[t][1]), d[t], sidx, 0, 0);
    SB();
    float e[4];
#pragma unroll
    for (int t = 0; t < 4; ++t) { const f32x2 h2 = f32x2{d[t][0], d[t][1]} + f32x2{d[t][2], d[t][3]}; e[t] = h2[0] + h2[1]; }
    const float y = tr16(tr32(e[0], e[2]), tr32(e[1], e[3]));
    __builtin_amdgcn_raw_buffer_store_b32(gpack(y, tgb), wsr, yso, ysb, SC1);
  }
  if (w == POLLW) {
    const unsigned ts = now();
    unsigned spins = 0;
    if (__builtin_amdgcn_ballot_w64(((gl[1] ^ tg0) | (gl[3] ^ tg1)) != 0u) != 0ull) {
#pragma nounroll
      do {
        MEMFENCE();
        gl = ldb<SC1>(wsr, lo, WS_L + grp * 1024);
      } while (__builtin_amdgcn_ballot_w64(((gl[1] ^ tg0) | (gl[3] ^ tg1)) != 0u) != 0ull && ++spins < SPIN_CAP);
    }
    const float la = __uint_as_float(gl[0]);
    const float wl = __builtin_amdgcn_rcpf(1.0f + __builtin_amdgcn_exp2f(dpp<XOR1>(la) - la));
    float wt[2];
#pragma unroll
    for (int i = 0; i < 2; ++i) wt[i] = __int_as_float(__builtin_amdgcn_ds_bpermute(((lane >> 1) + 32 * i) * 4, __float_as_int(wl)));
    int dl = (int)(gl[2] - ts), dm = dl;
    dl = max(dl, dppi<XOR1>(dl)); dl = max(dl, dppi<XOR2>(dl)); dl = max(dl, dppi<XOR7>(dl)); dl = max(dl, dppi<XOR8>(dl));
    dl = max(max(__builtin_amdgcn_readlane(dl, 0), __builtin_amdgcn_readlane(dl, 16)), max(__builtin_amdgcn_readlane(dl, 32), __builtin_amdgcn_readlane(dl, 48)));
    dm += dppi<XOR1>(dm); dm += dppi<XOR2>(dm); dm += dppi<XOR7>(dm); dm += dppi<XOR8>(dm);
    dm = ((__builtin_amdgcn_readlane(dm, 0) + __builtin_amdgcn_readlane(dm, 16)) + (__builtin_amdgcn_readlane(dm, 32) + __builtin_amdgcn_readlane(dm, 48))) >> 6;
    dl = min(max(max(dl + PLEAD, dm + PLMEAN), PMIN), 400);
    int yo = (lane >> 1) * 2048 + (lane & 1) * 16, yb = WS_Y + grp * 64 * 2048 + pidx * 32, yb1 = yb + 32 * 2048;
    unsigned want = (lane & 1) ? tg1 : tg0;
    asm volatile("" : "+s"(yb), "+s"(yb1));
    const rsrc_t outr = mkr(outp, 58);
    int oso = (lane & 14) ? OOB : (lane & 1) * 32 + (lane >> 4) * 8;
    asm volatile("" : "+v"(yo), "+v"(want), "+v"(oso), "+v"(wt[0]), "+v"(wt[1]));
    while ((int)(now() - ts) < dl) {}
#define YBAD ((((__builtin_amdgcn_perm(gy0[1], gy0[0], 0x0c0c0400u) | __builtin_amdgcn_perm(gy0[3], gy0[2], 0x04000c0cu)) ^ want) | \
              ((__builtin_amdgcn_perm(gy1[1], gy1[0], 0x0c0c0400u) | __builtin_amdgcn_perm(gy1[3], gy1[2], 0x04000c0cu)) ^ want)) != 0u)
    MEMFENCE();
    v4u gy0 = ldb<SC1>(wsr, yo, yb), gy1 = ldb<SC1>(wsr, yo, yb1);
    if (__builtin_expect(__builtin_amdgcn_ballot_w64(YBAD) != 0ull, 0)) {
#pragma nounroll
      do {
        MEMFENCE();
        gy0 = ldb<SC1>(wsr, yo, yb); gy1 = ldb<SC1>(wsr, yo, yb1);
      } while (__builtin_amdgcn_ballot_w64(YBAD) != 0ull && ++spins < SPIN_CAP);
    }
#undef YBAD
    const f32x2 a01 = __builtin_elementwise_fma(f32x2{wt[1], wt[1]}, f32x2{gval(gy1[0]), gval(gy1[1])}, wt[0] * f32x2{gval(gy0[0]), gval(gy0[1])});
    const f32x2 a23 = __builtin_elementwise_fma(f32x2{wt[1], wt[1]}, f32x2{gval(gy1[2]), gval(gy1[3])}, wt[0] * f32x2{gval(gy0[2]), gval(gy0[3])});
    const float a[4] = {a01[0], a01[1], a23[0], a23[1]};
    float z = tr16(tr32(a[0], a[2]), tr32(a[1], a[3]));
    z += dpp<XOR2>(z); z += dpp<ROR4>(z); z += dpp<XOR8>(z);
    __builtin_amdgcn_raw_buffer_store_b16((unsigned short)cvtpk(z, z), outr, oso, 0, 0);
    if (lane == 0 && pidx == 0 && b == 0) __builtin_amdgcn_raw_buffer_store_b64(v2u{tg0, tg1}, wsr, 0, WS_EPOCH + 256 * br, SC1);
  }
}

extern "C" void kernel_launch(void* const* d_in, const int* in_sizes, int n_in,
                              void* d_out, int out_size, void* d_ws, size_t ws_size,
                              hipStream_t stream) {
  Params P;
  for (int i = 0; i < 2; ++i) {
    P.x[i] = (const unsigned short*)d_in[5 * i + 0];
    P.k[i] = (const unsigned short*)d_in[5 * i + 1];
    P.v[i] = (const unsigned short*)d_in[5 * i + 2];
    P.wq[i] = (const unsigned short*)d_in[5 * i + 3];
    P.wo[i] = (const unsigned short*)d_in[5 * i + 4];
  }
  P.out = (unsigned short*)d_out;
  P.ws = (unsigned char*)d_ws;
  hipLaunchKernelGGL(k_fused, dim3(256), dim3(512), 0, stream, P);
}
```
